```python
import torch

def _softmax(x, axis=-1):
    return torch.softmax(x, dim=axis)

def _gelu(x):
    return torch.nn.functional.gelu(x, approximate='tanh')

def _silu(x):
    return torch.nn.functional.silu(x)

def _sigmoid(x):
    return torch.sigmoid(x)

def _layernorm(x, axis=-1, eps=1e-05):
    m = x.mean(dim=axis, keepdim=True)
    v = x.var(dim=axis, keepdim=True, unbiased=False)
    return (x - m) / torch.sqrt(v + eps)

def _scatter_add(tgt, idx, src, axis=0):
    return tgt.clone().index_add_(axis, idx, src)

def _segment_sum(src, seg, n):
    out = torch.zeros((n,) + src.shape[1:], dtype=src.dtype)
    return out.index_add_(0, seg, src)

def _pad(x, pw):
    flat = tuple((v for pair in reversed(pw) for v in pair))
    return torch.nn.functional.pad(x, flat)
kernel_name = 'adaLN_cond__sliding_local_dilated__dense_activation_silu__prompt'

def setup_inputs():

    _w = lambda a: (lambda t: t.to(torch.bfloat16) if t.is_floating_point() else t)(torch.as_tensor(a))

    def _A_setup():
        B, S, D = (1, 2048, 512)
        return {'x': _w((lambda *s: torch.randn(*s, dtype=torch.bfloat16) * 0.05)(B, S, D)), 'wqkv': _w((lambda *s: torch.randn(*s, dtype=torch.bfloat16) * 0.05)(D, 3 * D)), 'wo': _w((lambda *s: torch.randn(*s, dtype=torch.bfloat16) * 0.05)(D, D))}
    _A_in = _A_setup()

    def _B_setup():
        B, S, D, F = (1, 2048, 512, 1376)
        return {'x': _w((lambda *s: torch.randn(*s, dtype=torch.bfloat16) * 0.05)(B, S, D)), 'w1': _w((lambda *s: torch.randn(*s, dtype=torch.bfloat16) * 3.2)(D, F) * D ** (-0.5)), 'w2': _w((lambda *s: torch.randn(*s, dtype=torch.bfloat16) * 0.4)(F, D) * F ** (-0.5))}
    _B_in = _B_setup()
    return {'A_x': _A_in['x'], 'A_wqkv': _A_in['wqkv'], 'A_wo': _A_in['wo'], 'B_x': _B_in['x'], 'B_w1': _B_in['w1'], 'B_w2': _B_in['w2'], 'c': _w((lambda *s: torch.randn(*s, dtype=torch.bfloat16) * 0.1)(1, 512) * 8.0), 'w_mod': _w((lambda *s: torch.randn(*s, dtype=torch.bfloat16) * 0.1)(512, 6 * 512) * 512 ** (-0.5) * 8.0)}

def reference(A_x, A_wqkv, A_wo, B_x, B_w1, B_w2, c, w_mod):

    def _A_ref(x, wqkv, wo):
        B, S, D = x.shape
        H = 32
        Dh = D // H
        W, dil = (8, 2)
        qkv = (lambda x, *d: x.permute(*d))((x @ wqkv).reshape(B, S, 3, H, Dh), 2, 0, 3, 1, 4)
        q, k, v = (qkv[0], qkv[1], qkv[2])
        scale = Dh ** (-0.5)
        pos = torch.arange(S)
        off = torch.arange(W) * dil
        idx = pos[:, None] - off[None, :]
        mask = idx >= 0
        idx_c = torch.where(mask, idx, 0).reshape(-1)
        kg = k[:, :, idx_c, :].reshape(B, H, S, W, Dh)
        vg = v[:, :, idx_c, :].reshape(B, H, S, W, Dh)
        logits = torch.einsum('bhsd,bhswd->bhsw', q, kg) * scale
        logits = torch.where(mask[None, None, :, :], logits, -1000000000.0)
        attn = _softmax(logits, axis=-1)
        o = torch.einsum('bhsw,bhswd->bhsd', attn, vg)
        return (lambda x, *d: x.permute(*d))(o, 0, 2, 1, 3).reshape(B, S, D) @ wo

    def _B_ref(x, w1, w2):
        h = torch.einsum('bsd,df->bsf', x, w1)
        h = _silu(h)
        out = torch.einsum('bsf,fd->bsd', h, w2)
        return out
    x = A_x
    mod = (_silu(c) @ w_mod).reshape(c.shape[0], 6, 1, -1)
    sa, ca, ga, sb, cb, gb = (mod[:, 0].clone(), mod[:, 1].clone(), mod[:, 2].clone(), mod[:, 3].clone(), mod[:, 4].clone(), mod[:, 5].clone())
    h = _layernorm(x, axis=-1) * (1 + ca) + sa
    x = x + ga * _A_ref(h, A_wqkv, A_wo)
    h = _layernorm(x, axis=-1) * (1 + cb) + sb
    x = x + gb * _B_ref(h, B_w1, B_w2)
    return x
```

```cpp
#include <hip/hip_runtime.h>
#include <hip/hip_bf16.h>
#include <stdint.h>

#define LAS __attribute__((address_space(3)))
#define GAS __attribute__((address_space(1)))
typedef __bf16 bf16x8 __attribute__((ext_vector_type(8)));
typedef __bf16 bf16x4 __attribute__((ext_vector_type(4)));
typedef __bf16 bf16x2 __attribute__((ext_vector_type(2)));
typedef short s16x4 __attribute__((ext_vector_type(4)));
typedef float f32x4 __attribute__((ext_vector_type(4)));
typedef float f32x2 __attribute__((ext_vector_type(2)));
typedef unsigned u32x4 __attribute__((ext_vector_type(4)));
typedef unsigned u32x2 __attribute__((ext_vector_type(2)));
#define DI __device__ __forceinline__

constexpr size_t WS_MOD = 0, WS_O = 1u << 20, WS_Y = 4u << 20, WS_ST = 7u << 20, WS_X1 = 8u << 20, WS_U = 12u << 20;

DI unsigned pk2(float a, float b) { bf16x2 v = {(__bf16)a, (__bf16)b}; return __builtin_bit_cast(unsigned, v); }
DI float lo16(unsigned u) { return __uint_as_float(u << 16); }
DI float hi16(unsigned u) { return __uint_as_float(u & 0xffff0000u); }
DI float rbf(float a) { return (float)(__bf16)a; }
template <int CTRL> DI float dpp(float v) { return __int_as_float(__builtin_amdgcn_update_dpp(0, __float_as_int(v), CTRL, 0xf, 0xf, true)); }
template <int CTRL> DI unsigned dppu(unsigned v) { return (unsigned)__builtin_amdgcn_update_dpp(0, (int)v, CTRL, 0xf, 0xf, true); }
DI float sum8(float v) { v += dpp<0xB1>(v); v += dpp<0x4E>(v); v += dpp<0x141>(v); return v; }
DI float sum16(float v) { v = sum8(v); v += dpp<0x140>(v); return v; }
DI float xsum16(float v) { auto r = __builtin_amdgcn_permlane16_swap(__float_as_uint(v), __float_as_uint(v), false, false); return __uint_as_float(r[0]) + __uint_as_float(r[1]); }
DI float xsum32(float v) { auto r = __builtin_amdgcn_permlane32_swap(__float_as_uint(v), __float_as_uint(v), false, false); return __uint_as_float(r[0]) + __uint_as_float(r[1]); }
DI float xmax16(float v) { auto r = __builtin_amdgcn_permlane16_swap(__float_as_uint(v), __float_as_uint(v), false, false); return fmaxf(__uint_as_float(r[0]), __uint_as_float(r[1])); }
DI float xmax32(float v) { auto r = __builtin_amdgcn_permlane32_swap(__float_as_uint(v), __float_as_uint(v), false, false); return fmaxf(__uint_as_float(r[0]), __uint_as_float(r[1])); }
DI f32x4 mfma32(bf16x8 a, bf16x8 b, f32x4 c) { return __builtin_amdgcn_mfma_f32_16x16x32_bf16(a, b, c, 0, 0, 0); }
DI void dma16(const void* g, char* l) { __builtin_amdgcn_global_load_lds((const GAS void*)g, (LAS void*)l, 16, 0, 0); }
DI bf16x8 trfrag(const char* p0, const char* p1) {
    bf16x4 a = __builtin_amdgcn_ds_read_tr16_b64_v4bf16((LAS bf16x4*)p0);
    bf16x4 b = __builtin_amdgcn_ds_read_tr16_b64_v4bf16((LAS bf16x4*)p1);
    return __builtin_shufflevector(a, b, 0, 1, 2, 3, 4, 5, 6, 7);
}
DI void wg_map(int b, int& rg, int& cg) { const int xcd = b & 7, i = b >> 3; rg = xcd * 4 + (i >> 3); cg = i & 7; }

__global__ __launch_bounds__(256) void k_mod(const unsigned short* c, const unsigned short* wmod, float* mod) {
    __shared__ float red[32][64];
    __shared__ float sc[512];
    const int t = threadIdx.x;
    for (int i = t; i < 512; i += 256) { float v = lo16(c[i]); sc[i] = rbf(v / (1.f + __expf(-v))); }
    __syncthreads();
    const int ch = t & 7, r0 = t >> 3;
    float a[8] = {0, 0, 0, 0, 0, 0, 0, 0};
    for (int r = r0; r < 512; r += 32) {
        u32x4 v = *(const u32x4*)(wmod + (size_t)r * 3072 + blockIdx.x * 64 + ch * 8);
        const float s = sc[r];
#pragma unroll
        for (int j = 0; j < 4; ++j) { a[2 * j] += s * lo16(v[j]); a[2 * j + 1] += s * hi16(v[j]); }
    }
#pragma unroll
    for (int j = 0; j < 8; ++j) red[r0][ch * 8 + j] = a[j];
    __syncthreads();
    if (t < 64) { float s = 0; for (int i = 0; i < 32; ++i) s += red[i][t]; mod[blockIdx.x * 64 + t] = rbf(s); }
}

DI int act_off(int s, int g, int tok) { return (s * 64 + g * 16 + (tok ^ (g | ((s & 1) * 12)))) * 16; }

__global__ __launch_bounds__(512) void k_qkv(const char* x, const char* wqkv, char* ws) {
    extern __shared__ __attribute__((aligned(16))) char lds[];
    int rg, cg; wg_map(blockIdx.x, rg, cg);
    const int lane = threadIdx.x & 63, w = __builtin_amdgcn_readfirstlane(threadIdx.x >> 6);
    const int tk = lane & 15, g = lane >> 4, hd = w & 3, kh = w >> 2;
    const float* mod = (const float*)(ws + WS_MOD);
    char* STG = lds;
    char* XCH = lds + 96 * 1024;
    f32x2* STAT = (f32x2*)(lds + 120 * 1024);
    u32x4 xr[5][2];
    {
        const int r8 = lane >> 3, c8 = lane & 7;
#pragma unroll
        for (int T = 0; T < 5; ++T)
#pragma unroll
            for (int i = 0; i < 2; ++i) {
                int row = rg * 64 - 16 + T * 16 + i * 8 + r8; if (row < 0) row += 16;
                xr[T][i] = *(const u32x4*)(x + (size_t)row * 1024 + w * 128 + c8 * 16);
            }
    }
    bf16x8 W[8][3];
    {
        const int r8 = lane >> 3, cp = lane & 7;
        const int q = (lane & 15) >> 2, p = lane & 3;
        const int f = ((q >> 1) & 1) | ((g & 1) << 1);
        const int rdoff = (8 * g + q) * 128 + (((2 * hd) ^ (f << 1)) + (p >> 1)) * 16 + (p & 1) * 8;
#pragma unroll
        for (int j = 0; j < 2; ++j) {
            const int ks = (w >> 2) * 8 + 4 * j + (w & 3);
#pragma unroll
            for (int m = 0; m < 3; ++m)
#pragma unroll
                for (int i = 0; i < 4; ++i) {
                    const int row = i * 8 + r8; const int fr = ((row >> 1) & 1) | (((row >> 3) & 1) << 1);
                    dma16(wqkv + (size_t)(ks * 32 + row) * 3072 + m * 1024 + cg * 128 + (cp ^ (fr << 1)) * 16, STG + (w * 3 + m) * 4096 + i * 1024);
                }
            asm volatile("s_waitcnt vmcnt(0)" ::: "memory");
            __syncthreads();
#pragma unroll
            for (int i = 0; i < 4; ++i)
#pragma unroll
                for (int m = 0; m < 3; ++m) {
                    const char* b = STG + ((kh * 4 + i) * 3 + m) * 4096 + rdoff;
                    W[4 * j + i][m] = trfrag(b, b + 512);
                }
            __syncthreads();
        }
    }
    const int r8 = lane >> 3, c8 = lane & 7;
#pragma unroll
    for (int T = 0; T < 5; ++T)
#pragma unroll
        for (int i = 0; i < 2; ++i) {
            float v[8];
#pragma unroll
            for (int j = 0; j < 4; ++j) { v[2 * j] = lo16(xr[T][i][j]); v[2 * j + 1] = hi16(xr[T][i][j]); }
            float s = ((v[0] + v[1]) + (v[2] + v[3])) + ((v[4] + v[5]) + (v[6] + v[7]));
            const float m = sum8(s) * (1.f / 64.f);
            float q = 0;
#pragma unroll
            for (int j = 0; j < 8; ++j) { const float d = v[j] - m; q += d * d; }
            q = sum8(q);
            if (c8 == 0) STAT[(T * 16 + i * 8 + r8) * 8 + w] = (f32x2){m, q};
        }
    __syncthreads();
    float A[8], S[8];
#pragma unroll
    for (int j = 0; j < 8; ++j) { S[j] = mod[w * 64 + c8 * 8 + j]; A[j] = rbf(1.f + mod[512 + w * 64 + c8 * 8 + j]); }
#pragma unroll
    for (int T = 0; T < 5; ++T)
#pragma unroll
        for (int i = 0; i < 2; ++i) {
            const f32x2 st = STAT[(T * 16 + i * 8 + r8) * 8 + c8];
            const float mean = sum8(st.x) * 0.125f; const float dm = st.x - mean;
            const float m2 = sum8(st.y + 64.f * dm * dm);
            const float rs = __builtin_amdgcn_rsqf(m2 * (1.f / 512.f) + 1e-5f);
            u32x4 h;
#pragma unroll
            for (int j = 0; j < 4; ++j) {
                const float a = (lo16(xr[T][i][j]) - mean) * rs, b = (hi16(xr[T][i][j]) - mean) * rs;
                h[j] = pk2(a * A[2 * j] + S[2 * j], b * A[2 * j + 1] + S[2 * j + 1]);
            }
            *(u32x4*)(STG + T * 16384 + act_off(2 * w + (c8 >> 2), c8 & 3, i * 8 + r8)) = h;
        }
    __syncthreads();
    f32x4 mcur, mprev; unsigned M[4];
    {
        const float NI = -__builtin_inff();
        bool vc[4], vp[4];
#pragma unroll
        for (int r = 0; r < 4; ++r) { const int d = tk - (4 * g + r); vc[r] = d >= 0 && !(d & 1); vp[r] = d <= -2 && !(d & 1); mcur[r] = vc[r] ? 0.f : NI; mprev[r] = vp[r] ? 0.f : NI; }
        M[0] = (vp[0] ? 0xffffu : 0u) | (vp[1] ? 0xffff0000u : 0u); M[1] = (vp[2] ? 0xffffu : 0u) | (vp[3] ? 0xffff0000u : 0u);
        M[2] = (vc[0] ? 0xffffu : 0u) | (vc[1] ? 0xffff0000u : 0u); M[3] = (vc[2] ? 0xffffu : 0u) | (vc[3] ? 0xffff0000u : 0u);
    }
    char* xp = XCH + hd * 5120;
#pragma unroll
    for (int T = 0; T < 5; ++T) {
        f32x4 aq = {0, 0, 0, 0}, ak = {0, 0, 0, 0}, av = {0, 0, 0, 0};
#pragma unroll
        for (int s = 0; s < 8; ++s) {
            const bf16x8 hf = *(const bf16x8*)(STG + T * 16384 + act_off(kh * 8 + s, g, tk));
            if (T > 0) aq = mfma32(W[s][0], hf, aq);
            ak = mfma32(W[s][1], hf, ak);
            av = mfma32(hf, W[s][2], av);
        }
        const bool fin = kh == (T & 1);
        if (!fin) {
            if (T > 0) *(f32x4*)(xp + lane * 16) = aq;
            *(f32x4*)(xp + 1024 + lane * 16) = ak; *(f32x4*)(xp + 2048 + lane * 16) = av;
        }
        __syncthreads();
        if (fin) {
            if (T > 0) aq += *(const f32x4*)(xp + lane * 16);
            ak += *(const f32x4*)(xp + 1024 + lane * 16); av += *(const f32x4*)(xp + 2048 + lane * 16);
            u32x4 kv = {pk2(ak[0], ak[1]), pk2(ak[2], ak[3]), pk2(av[0], av[1]), pk2(av[2], av[3])};
            *(u32x4*)(xp + 3072 + (T & 1) * 1024 + lane * 16) = kv;
            if (T > 0) {
                const u32x4 kvp = *(const u32x4*)(xp + 3072 + ((T - 1) & 1) * 1024 + lane * 16);
                const u32x2 qq = {pk2(aq[0], aq[1]), pk2(aq[2], aq[3])};
                f32x4 mp = mprev; if (T == 1 && rg == 0) { const float NI = -__builtin_inff(); mp = (f32x4){NI, NI, NI, NI}; }
                const f32x4 sp = __builtin_amdgcn_mfma_f32_16x16x16bf16_1k(__builtin_bit_cast(s16x4, (u32x2){kvp[0], kvp[1]}), __builtin_bit_cast(s16x4, qq), mp, 0, 0, 0);
                const f32x4 sc = __builtin_amdgcn_mfma_f32_16x16x16bf16_1k(__builtin_bit_cast(s16x4, (u32x2){kv[0], kv[1]}), __builtin_bit_cast(s16x4, qq), mcur, 0, 0, 0);
                const float t0 = fmaxf(fmaxf(sp[0], sp[1]), fmaxf(sc[0], sc[1])), t1 = fmaxf(fmaxf(sp[2], sp[3]), fmaxf(sc[2], sc[3]));
                const float mx = xmax32(xmax16(fmaxf(t0, t1)));
                const float C = 0.25f * 1.44269504089f;
                const float e0 = __builtin_amdgcn_exp2f((t0 - mx) * C), e1 = __builtin_amdgcn_exp2f((t1 - mx) * C);
                const float inv = __builtin_amdgcn_rcpf(xsum32(xsum16(e0 + e1)));
                const unsigned p0 = pk2(e0 * inv, e0 * inv), p1 = pk2(e1 * inv, e1 * inv);
                const u32x4 P = {p0 & M[0], p1 & M[1], p0 & M[2], p1 & M[3]};
                const u32x4 V = {kvp[2], kvp[3], kv[2], kv[3]};
                const f32x4 o = mfma32(__builtin_bit_cast(bf16x8, V), __builtin_bit_cast(bf16x8, P), (f32x4){0, 0, 0, 0});
                const unsigned oa = pk2(o[0], o[1]), ob = pk2(o[2], o[3]);
                auto ra = __builtin_amdgcn_permlane16_swap(oa, oa, false, false);
                auto rb = __builtin_amdgcn_permlane16_swap(ob, ob, false, false);
                const int H = cg * 4 + hd, tile = rg * 4 + T - 1;
                if (!(g & 1)) *(u32x4*)(ws + WS_O + ((size_t)(tile * 16 + (H >> 1)) * 64 + ((H & 1) * 2 + (g >> 1)) * 16 + tk) * 16) = (u32x4){ra[0], rb[0], ra[1], rb[1]};
            }
        }
        __syncthreads();
    }
}

DI int pb_off(int w, int c, int g, int tok) { return w * 4352 + c * 1088 + g * 272 + tok * 16; }

__global__ __launch_bounds__(512) void k_wo(const char* x, const char* wo, char* ws) {
    extern __shared__ __attribute__((aligned(16))) char lds[];
    int rg, cg; wg_map(blockIdx.x, rg, cg);
    const int lane = threadIdx.x & 63, w = __builtin_amdgcn_readfirstlane(threadIdx.x >> 6);
    const int tk = lane & 15, g = lane >> 4;
    const float* mod = (const float*)(ws + WS_MOD);
    char* STG = lds + w * 8192; char* PB = lds + 64 * 1024;
    bf16x8 W[2][4];
    {
        const int r8 = lane >> 3, cp = lane & 7, q = (lane & 15) >> 2, p = lane & 3;
        const int f = ((q >> 1) & 1) | ((g & 1) << 1);
#pragma unroll
        for (int i = 0; i < 8; ++i) {
            const int row = i * 8 + r8; const int fr = ((row >> 1) & 1) | (((row >> 3) & 1) << 1);
            dma16(wo + (size_t)(w * 64 + row) * 1024 + cg * 128 + (cp ^ (fr << 1)) * 16, STG + i * 1024);
        }
        asm volatile("s_waitcnt vmcnt(0)" ::: "memory");
#pragma unroll
        for (int s = 0; s < 2; ++s)
#pragma unroll
            for (int c = 0; c < 4; ++c) {
                const char* b = STG + s * 4096 + (8 * g + q) * 128 + (((2 * c) ^ (f << 1)) + (p >> 1)) * 16 + (p & 1) * 8;
                W[s][c] = trfrag(b, b + 512);
            }
    }
    const int ft = 2 * w + (lane >> 5), fs = (lane >> 4) & 1, fg = (lane >> 2) & 3, fd = lane & 3;
    const int fc = 2 * fs + (fd >> 1), fcol = cg * 64 + fc * 16 + fg * 4 + (fd & 1) * 2;
    const float ga0 = mod[1024 + fcol], ga1 = mod[1024 + fcol + 1];
    const float A0 = rbf(1.f + mod[2048 + fcol]), A1 = rbf(1.f + mod[2048 + fcol + 1]);
#pragma unroll
    for (int t = 0; t < 4; ++t) {
        const int tile = rg * 4 + t;
        f32x4 acc[4];
#pragma unroll
        for (int c = 0; c < 4; ++c) acc[c] = (f32x4){0, 0, 0, 0};
#pragma unroll
        for (int s = 0; s < 2; ++s) {
            const bf16x8 of = *(const bf16x8*)(ws + WS_O + ((size_t)(tile * 16 + 2 * w + s) * 64 + lane) * 16);
#pragma unroll
            for (int c = 0; c < 4; ++c) acc[c] = mfma32(W[s][c], of, acc[c]);
        }
#pragma unroll
        for (int c = 0; c < 4; ++c) *(f32x4*)(PB + pb_off(w, c, g, tk)) = acc[c];
        __syncthreads();
        f32x2 sum = {0, 0};
#pragma unroll
        for (int ww = 0; ww < 8; ++ww) sum += *(const f32x2*)(PB + pb_off(ww, fc, fg, ft) + (fd & 1) * 8);
        __syncthreads();
        const int row = tile * 16 + ft;
        const unsigned xv = *(const unsigned*)(x + (size_t)row * 1024 + fcol * 2);
        const unsigned x1 = pk2(lo16(xv) + ga0 * sum[0], hi16(xv) + ga1 * sum[1]);
        const float a = lo16(x1), b = hi16(x1);
        *(unsigned*)(ws + WS_X1 + (size_t)row * 1024 + fcol * 2) = x1;
        const float m = xsum16(sum16(a + b)) * (1.f / 64.f);
        const float q2 = xsum16(sum16((a - m) * (a - m) + (b - m) * (b - m)));
        if ((lane & 31) == 0) *(f32x2*)(ws + WS_ST + ((size_t)row * 8 + cg) * 8) = (f32x2){m, q2};
        const unsigned y = pk2(a * A0, b * A1);
        const u32x4 yy = {y, dppu<0x55>(y), dppu<0xAA>(y), dppu<0xFF>(y)};
        if (fd == 0) *(u32x4*)(ws + WS_Y + (size_t)row * 1024 + cg * 128 + (fs * 4 + fg) * 16) = yy;
    }
}

__global__ __launch_bounds__(512) void k_f1(const char* w1, char* ws) {
    extern __shared__ __attribute__((aligned(16))) char lds[];
    int rg, cg; wg_map(blockIdx.x, rg, cg);
    const int lane = threadIdx.x & 63, w = __builtin_amdgcn_readfirstlane(threadIdx.x >> 6);
    const int tk = lane & 15, g = lane >> 4, cp4 = w & 3, kh = w >> 2;
    const float* mod = (const float*)(ws + WS_MOD);
    const int tile0 = cg * 11 > 75 ? 75 : cg * 11;
    char* STG = lds;
    char* ACT = lds + 96 * 1024;
    char* XCH = lds + 112 * 1024;
    f32x2* SST = (f32x2*)(lds + 128 * 1024);
    bf16x8 W[8][3];
    {
        const int q = (lane & 15) >> 2, p = lane & 3;
#pragma unroll
        for (int j = 0; j < 2; ++j) {
            const int ks = (w >> 2) * 8 + 4 * j + (w & 3);
#pragma unroll
            for (int i = 0; i < 11; ++i) {
                const int ch = i * 64 + lane, r = ch / 22, c = ch - r * 22;
                dma16(w1 + (size_t)(ks * 32 + r) * 2752 + tile0 * 32 + c * 16, STG + w * 11264 + i * 1024);
            }
            asm volatile("s_waitcnt vmcnt(0)" ::: "memory");
            __syncthreads();
#pragma unroll
            for (int i = 0; i < 4; ++i)
#pragma unroll
                for (int m = 0; m < 3; ++m) {
                    int ct = cp4 * 3 + m; if (ct > 10) ct = 10;
                    const char* b = STG + (kh * 4 + i) * 11264 + (4 * g + q) * 352 + ct * 32 + p * 8;
                    W[4 * j + i][m] = trfrag(b, b + 16 * 352);
                }
            __syncthreads();
        }
    }
    const int r8 = lane >> 3, c8 = lane & 7;
    f32x4 s1[3], t1[3];
    char* xp = XCH + cp4 * 3072;
#pragma unroll
    for (int t = -1; t < 4; ++t) {
        const int tile = rg * 4 + t;
        if (t < 0) {
            u32x4 v = {0, 0, 0, 0};
            const int k0 = w * 64 + (c8 >> 2) * 32 + (c8 & 3) * 4;
            if (r8 == 0) v = (u32x4){pk2(1.f + mod[2048 + k0], 1.f + mod[2048 + k0 + 1]), pk2(1.f + mod[2048 + k0 + 2], 1.f + mod[2048 + k0 + 3]),
                                     pk2(1.f + mod[2048 + k0 + 16], 1.f + mod[2048 + k0 + 17]), pk2(1.f + mod[2048 + k0 + 18], 1.f + mod[2048 + k0 + 19])};
            if (r8 == 1) v = (u32x4){pk2(mod[1536 + k0], mod[1536 + k0 + 1]), pk2(mod[1536 + k0 + 2], mod[1536 + k0 + 3]),
                                     pk2(mod[1536 + k0 + 16], mod[1536 + k0 + 17]), pk2(mod[1536 + k0 + 18], mod[1536 + k0 + 19])};
            *(u32x4*)(ACT + act_off(2 * w + (c8 >> 2), c8 & 3, r8)) = v;
            *(u32x4*)(ACT + act_off(2 * w + (c8 >> 2), c8 & 3, 8 + r8)) = (u32x4){0, 0, 0, 0};
        } else {
#pragma unroll
            for (int i = 0; i < 2; ++i) {
                const u32x4 v = *(const u32x4*)(ws + WS_Y + (size_t)(tile * 16 + i * 8 + r8) * 1024 + w * 128 + c8 * 16);
                *(u32x4*)(ACT + act_off(2 * w + (c8 >> 2), c8 & 3, i * 8 + r8)) = v;
            }
            if (w < 2) {
                const f32x2 st = *(const f32x2*)(ws + WS_ST + ((size_t)(tile * 16 + w * 8 + r8) * 8 + c8) * 8);
                const float mean = sum8(st.x) * 0.125f; const float dm = st.x - mean;
                const float m2 = sum8(st.y + 64.f * dm * dm);
                if (c8 == 0) SST[w * 8 + r8] = (f32x2){mean, __builtin_amdgcn_rsqf(m2 * (1.f / 512.f) + 1e-5f)};
            }
        }
        __syncthreads();
        f32x4 acc[3];
#pragma unroll
        for (int m = 0; m < 3; ++m) acc[m] = (f32x4){0, 0, 0, 0};
#pragma unroll
        for (int s = 0; s < 8; ++s) {
            const bf16x8 yf = *(const bf16x8*)(ACT + act_off(kh * 8 + s, g, tk));
#pragma unroll
            for (int m = 0; m < 3; ++m) acc[m] = mfma32(W[s][m], yf, acc[m]);
        }
        const f32x2 st = SST[tk];
        if (kh == 1) { *(f32x4*)(xp + lane * 16) = acc[0]; *(f32x4*)(xp + 1024 + lane * 16) = acc[1]; }
        else *(f32x4*)(xp + 2048 + lane * 16) = acc[2];
        __syncthreads();
        if (kh == 0) { acc[0] += *(const f32x4*)(xp + lane * 16); acc[1] += *(const f32x4*)(xp + 1024 + lane * 16); }
        else acc[2] += *(const f32x4*)(xp + 2048 + lane * 16);
        if (t < 0) {
#pragma unroll
            for (int m = 0; m < 3; ++m)
#pragma unroll
                for (int r = 0; r < 4; ++r) { s1[m][r] = dpp<0x150>(acc[m][r]); t1[m][r] = dpp<0x151>(acc[m][r]); }
        } else {
            unsigned ua[3], ub[3];
#pragma unroll
            for (int m = 0; m < 3; ++m) {
                float v[4];
#pragma unroll
                for (int r = 0; r < 4; ++r) { const float z = st.y * (acc[m][r] - st.x * s1[m][r]) + t1[m][r]; v[r] = z * __builtin_amdgcn_rcpf(1.f + __builtin_amdgcn_exp2f(-1.44269504089f * z)); }
                ua[m] = pk2(v[0], v[1]); ub[m] = pk2(v[2], v[3]);
            }
            if (kh == 0) {
                auto ra = __builtin_amdgcn_permlane16_swap(ua[0], ua[1], false, false);
                auto rb = __builtin_amdgcn_permlane16_swap(ub[0], ub[1], false, false);
                const int ct = tile0 + cp4 * 3 + (g & 1);
                *(u32x4*)(ws + WS_U + ((size_t)(tile * 43 + (ct >> 1)) * 64 + ((ct & 1) * 2 + (g >> 1)) * 16 + tk) * 16) = (u32x4){ra[0], rb[0], ra[1], rb[1]};
            } else if (cp4 < 3) {
                auto ra = __builtin_amdgcn_permlane16_swap(ua[2], ua[2], false, false);
                auto rb = __builtin_amdgcn_permlane16_swap(ub[2], ub[2], false, false);
                const int ct = tile0 + cp4 * 3 + 2;
                if (!(g & 1)) *(u32x4*)(ws + WS_U + ((size_t)(tile * 43 + (ct >> 1)) * 64 + ((ct & 1) * 2 + (g >> 1)) * 16 + tk) * 16) = (u32x4){ra[0], rb[0], ra[1], rb[1]};
            }
        }
        __syncthreads();
    }
}

__global__ __launch_bounds__(512) void k_f2(const char* w2, char* ws, char* out) {
    extern __shared__ __attribute__((aligned(16))) char lds[];
    int rg, cg; wg_map(blockIdx.x, rg, cg);
    const int lane = threadIdx.x & 63, w = __builtin_amdgcn_readfirstlane(threadIdx.x >> 6);
    const int tk = lane & 15, g = lane >> 4;
    const float* mod = (const float*)(ws + WS_MOD);
    char* STG = lds + w * 12288; char* PB = lds + 96 * 1024;
    const int ks0 = w * 5 + (w < 3 ? w : 3), nks = w < 3 ? 6 : 5;
    bf16x8 W[6][4];
    {
        const int r8 = lane >> 3, cp = lane & 7, q = (lane & 15) >> 2, p = lane & 3;
        const int f = ((q >> 1) & 1) | ((g & 1) << 1);
#pragma unroll
        for (int j = 0; j < 2; ++j) {
#pragma unroll
            for (int i = 0; i < 12; ++i) {
                int row = (ks0 + 3 * j) * 32 + i * 8 + r8; const int fr = ((row >> 1) & 1) | (((row >> 3) & 1) << 1);
                if (row >= 1376) row -= 32;
                dma16(w2 + (size_t)row * 1024 + cg * 128 + (cp ^ (fr << 1)) * 16, STG + i * 1024);
            }
            asm volatile("s_waitcnt vmcnt(0)" ::: "memory");
#pragma unroll
            for (int s = 0; s < 3; ++s)
#pragma unroll
                for (int c = 0; c < 4; ++c) {
                    const char* b = STG + s * 4096 + (8 * g + q) * 128 + (((2 * c) ^ (f << 1)) + (p >> 1)) * 16 + (p & 1) * 8;
                    W[3 * j + s][c] = trfrag(b, b + 512);
                }
            asm volatile("s_waitcnt lgkmcnt(0)" ::: "memory");
        }
    }
    const int ft = 2 * w + (lane >> 5), fs = (lane >> 4) & 1, fg = (lane >> 2) & 3, fd = lane & 3;
    const int fc = 2 * fs + (fd >> 1), fcol = cg * 64 + fc * 16 + fg * 4 + (fd & 1) * 2;
    const float gb0 = mod[2560 + fcol], gb1 = mod[2560 + fcol + 1];
#pragma unroll
    for (int t = 0; t < 4; ++t) {
        const int tile = rg * 4 + t;
        f32x4 acc[4];
#pragma unroll
        for (int c = 0; c < 4; ++c) acc[c] = (f32x4){0, 0, 0, 0};
#pragma unroll
        for (int s = 0; s < 6; ++s) {
            if (s < nks) {
                const bf16x8 uf = *(const bf16x8*)(ws + WS_U + ((size_t)(tile * 43 + ks0 + s) * 64 + lane) * 16);
#pragma unroll
                for (int c = 0; c < 4; ++c) acc[c] = mfma32(W[s][c], uf, acc[c]);
            }
        }
#pragma unroll
        for (int c = 0; c < 4; ++c) *(f32x4*)(PB + pb_off(w, c, g, tk)) = acc[c];
        __syncthreads();
        f32x2 sum = {0, 0};
#pragma unroll
        for (int ww = 0; ww < 8; ++ww) sum += *(const f32x2*)(PB + pb_off(ww, fc, fg, ft) + (fd & 1) * 8);
        __syncthreads();
        const int row = tile * 16 + ft;
        const unsigned x1 = *(const unsigned*)(ws + WS_X1 + (size_t)row * 1024 + fcol * 2);
        *(unsigned*)(out + (size_t)row * 1024 + fcol * 2) = pk2(lo16(x1) + gb0 * sum[0], hi16(x1) + gb1 * sum[1]);
    }
}

extern "C" void kernel_launch(void* const* d_in, const int* in_sizes, int n_in, void* d_out, int out_size, void* d_ws, size_t ws_size, hipStream_t stream) {
    const char* x = (const char*)d_in[0]; const char* wqkv = (const char*)d_in[1]; const char* wo = (const char*)d_in[2];
    const char* w1 = (const char*)d_in[4]; const char* w2 = (const char*)d_in[5];
    char* ws = (char*)d_ws;
    const int L = 144 * 1024;
    (void)hipFuncSetAttribute((const void*)k_qkv, hipFuncAttributeMaxDynamicSharedMemorySize, L);
    (void)hipFuncSetAttribute((const void*)k_wo, hipFuncAttributeMaxDynamicSharedMemorySize, L);
    (void)hipFuncSetAttribute((const void*)k_f1, hipFuncAttributeMaxDynamicSharedMemorySize, L);
    (void)hipFuncSetAttribute((const void*)k_f2, hipFuncAttributeMaxDynamicSharedMemorySize, L);
    k_mod<<<48, 256, 0, stream>>>((const unsigned short*)d_in[6], (const unsigned short*)d_in[7], (float*)(ws + WS_MOD));
    k_qkv<<<256, 512, L, stream>>>(x, wqkv, ws);
    k_wo<<<256, 512, L, stream>>>(x, wo, ws);
    k_f1<<<256, 512, L, stream>>>(w1, ws);
    k_f2<<<256, 512, L, stream>>>(w2, ws, (char*)d_out);
}
```

```cpp
#include <hip/hip_runtime.h>
#include <stdint.h>

#define LAS __attribute__((address_space(3)))
typedef __bf16 bf16x8 __attribute__((ext_vector_type(8)));
typedef __bf16 bf16x4 __attribute__((ext_vector_type(4)));
typedef __bf16 bf16x2 __attribute__((ext_vector_type(2)));
typedef short s16x4 __attribute__((ext_vector_type(4)));
typedef float f32x4 __attribute__((ext_vector_type(4)));
typedef float f32x2 __attribute__((ext_vector_type(2)));
typedef unsigned u32x4 __attribute__((ext_vector_type(4)));
typedef unsigned u32x2 __attribute__((ext_vector_type(2)));
typedef __amdgpu_buffer_rsrc_t rsrc_t;
#define DI __device__ __forceinline__
#define MAP 1
#define ROT 1

constexpr unsigned WS_EPOCH = 0, WS_FLAG = 1024, WS_MODP = 4096, WS_ST = 65536, WS_O = 1u << 20, WS_Y = 3u << 20, WS_U = 5u << 20, WS_BYTES = 11u << 20;
constexpr int SPIN = 1 << 15;
constexpr unsigned K = 1024;
constexpr unsigned L_M = 120 * K, L_STAT = 144 * K, L_RST = 149 * K, L_SC = 150 * K, L_AS = 152 * K, L_XCH = 144 * K;
constexpr unsigned L_CB = 64 * K, L_GT = 66 * K, L_SST = 67 * K, L_S1P = 68 * K, L_S1 = 72 * K;
constexpr unsigned L_ACT = 74 * K, L_XC1 = 106 * K, L_W2A = 128 * K;

DI unsigned pk2(float a, float b) { bf16x2 v = {(__bf16)a, (__bf16)b}; return __builtin_bit_cast(unsigned, v); }
DI float lo16(unsigned u) { return __uint_as_float(u << 16); }
DI float hi16(unsigned u) { return __uint_as_float(u & 0xffff0000u); }
template <int CTRL> DI float dpp(float v) { return __int_as_float(__builtin_amdgcn_update_dpp(0, __float_as_int(v), CTRL, 0xf, 0xf, true)); }
template <int CTRL> DI unsigned dppu(unsigned v) { return (unsigned)__builtin_amdgcn_update_dpp(0, (int)v, CTRL, 0xf, 0xf, true); }
DI float fmx(float a, float b) { return __builtin_elementwise_maximum(a, b); }
DI float dot2(unsigned a, unsigned b, float c) { return __builtin_amdgcn_fdot2_f32_bf16(__builtin_bit_cast(bf16x2, a), __builtin_bit_cast(bf16x2, b), c, false); }
DI float sw16add(float a, float b) { auto r = __builtin_amdgcn_permlane16_swap(__float_as_uint(a), __float_as_uint(b), false, false); return __uint_as_float(r[0]) + __uint_as_float(r[1]); }
DI float sw32add(float a, float b) { auto r = __builtin_amdgcn_permlane32_swap(__float_as_uint(a), __float_as_uint(b), false, false); return __uint_as_float(r[0]) + __uint_as_float(r[1]); }
DI float xmax16(float v) { auto r = __builtin_amdgcn_permlane16_swap(__float_as_uint(v), __float_as_uint(v), false, false); return fmx(__uint_as_float(r[0]), __uint_as_float(r[1])); }
DI float xmax32(float v) { auto r = __builtin_amdgcn_permlane32_swap(__float_as_uint(v), __float_as_uint(v), false, false); return fmx(__uint_as_float(r[0]), __uint_as_float(r[1])); }
DI f32x4 mfma32(bf16x8 a, bf16x8 b, f32x4 c) { return __builtin_amdgcn_mfma_f32_16x16x32_bf16(a, b, c, 0, 0, 0); }
DI f32x4 mfma32u(u32x4 a, u32x4 b, f32x4 c) { return mfma32(__builtin_bit_cast(bf16x8, a), __builtin_bit_cast(bf16x8, b), c); }
DI f32x4 mfma16u(unsigned a0, unsigned a1, unsigned b0, unsigned b1, f32x4 c) {
    return __builtin_amdgcn_mfma_f32_16x16x16bf16_1k(__builtin_bit_cast(s16x4, (u32x2){a0, a1}), __builtin_bit_cast(s16x4, (u32x2){b0, b1}), c, 0, 0, 0);
}
DI rsrc_t mk(const void* p, unsigned n) { return __builtin_amdgcn_make_buffer_rsrc((void*)p, (short)0, (int)n, 0x00020000); }
DI unsigned nosent(unsigned v) { return v < 0xfffffffeu ? v : 0xfffffffeu; }
DI unsigned umax3(unsigned a, unsigned b, unsigned c) { unsigned m = a > b ? a : b; return m > c ? m : c; }
DI unsigned umax4(u32x4 v, unsigned m) { return umax3(umax3(v[0], v[1], m), v[2], v[3]); }

#define BAR() asm volatile("s_waitcnt lgkmcnt(0)\n\ts_barrier" ::: "memory")
#define VMW(n) asm volatile("s_waitcnt vmcnt(%0)" ::"n"(n) : "memory")
#define TIE1(a) asm volatile("; TIE %0" : "+v"(a))
#define TIE2(a, b) asm volatile("; TIE %0 %1" : "+v"(a), "+v"(b))
DI void dma1(rsrc_t r, unsigned m0, unsigned vo, unsigned so) {
    asm volatile("s_mov_b32 m0, %0\n\ts_nop 0\n\tbuffer_load_dwordx4 %1, %2, %3 offen lds" ::"s"(m0), "v"(vo), "s"(r), "s"(so) : "m0", "memory");
}
DI void dma3(rsrc_t r, unsigned m0, unsigned vo, unsigned so) {
    asm volatile("s_mov_b32 m0, %0\n\ts_nop 0\n\tbuffer_load_dwordx4 %1, %2, %3 offen lds\n\tbuffer_load_dwordx4 %1, %2, %3 offen offset:1024 lds\n\tbuffer_load_dwordx4 %1, %2, %3 offen offset:2048 lds"
                 ::"s"(m0), "v"(vo), "s"(r), "s"(so) : "m0", "memory");
}
DI void dma4(rsrc_t r, unsigned m0, unsigned vo0, unsigned vo1, unsigned so) {
    asm volatile("s_mov_b32 m0, %0\n\ts_nop 0\n\tbuffer_load_dwordx4 %1, %3, %4 offen lds\n\tbuffer_load_dwordx4 %2, %3, %5 offen offset:1024 lds\n\t"
                 "buffer_load_dwordx4 %1, %3, %6 offen offset:2048 lds\n\tbuffer_load_dwordx4 %2, %3, %7 offen offset:3072 lds"
                 ::"s"(m0), "v"(vo0), "v"(vo1), "s"(r), "s"(so), "s"(so + 7168u), "s"(so + 14336u), "s"(so + 21504u) : "m0", "memory");
}
template <int OFF> DI u32x4 ld16(rsrc_t r, unsigned vo, unsigned so) {
    u32x4 v; asm volatile("buffer_load_dwordx4 %0, %1, %2, %3 offen offset:%4" : "=v"(v) : "v"(vo), "s"(r), "s"(so), "n"(OFF) : "memory"); return v;
}
template <int OFF> DI u32x4 ld16c(rsrc_t r, unsigned vo, unsigned so) {
    u32x4 v; asm volatile("buffer_load_dwordx4 %0, %1, %2, %3 offen offset:%4 sc1" : "=v"(v) : "v"(vo), "s"(r), "s"(so), "n"(OFF) : "memory"); return v;
}
DI u32x2 ld8(rsrc_t r, unsigned vo, unsigned so) {
    u32x2 v; asm volatile("buffer_load_dwordx2 %0, %1, %2, %3 offen" : "=v"(v) : "v"(vo), "s"(r), "s"(so) : "memory"); return v;
}
DI unsigned ld4(rsrc_t r, unsigned vo, unsigned so) {
    unsigned v; asm volatile("buffer_load_dword %0, %1, %2, %3 offen" : "=v"(v) : "v"(vo), "s"(r), "s"(so) : "memory"); return v;
}
DI unsigned ld4c(rsrc_t r, unsigned vo, unsigned so) {
    unsigned v; asm volatile("buffer_load_dword %0, %1, %2, %3 offen sc1" : "=v"(v) : "v"(vo), "s"(r), "s"(so) : "memory"); return v;
}
DI void st16c(rsrc_t r, unsigned vo, u32x4 v) { __builtin_amdgcn_raw_buffer_store_b128(v, r, (int)vo, 0, 16); }
DI void st4c(rsrc_t r, unsigned vo, unsigned v) { __builtin_amdgcn_raw_buffer_store_b32(v, r, (int)vo, 0, 16); }
DI bool anyl(bool c) { return __builtin_amdgcn_ballot_w64(c) != 0; }

#define STAMP(i)

__global__ __launch_bounds__(512) void fused_block(const char* x, const char* wqkv, const char* wo, const char* w1, const char* w2,
                                                   const char* cvec, const char* wmod, char* ws, char* out) {
    __shared__ __attribute__((aligned(16))) char lds[163840];
    LAS char* L = (LAS char*)lds;
    const unsigned LB = (unsigned)(uintptr_t)L;
#define LD(T, off) (*(LAS T*)(L + (off)))
#define TRF(off, d) __builtin_shufflevector(__builtin_amdgcn_ds_read_tr16_b64_v4bf16((LAS bf16x4*)(L + (off))), __builtin_amdgcn_ds_read_tr16_b64_v4bf16((LAS bf16x4*)(L + (off) + (d))), 0, 1, 2, 3, 4, 5, 6, 7)
    const unsigned lane = threadIdx.x & 63, w = __builtin_amdgcn_readfirstlane(threadIdx.x >> 6);
    const unsigned b = blockIdx.x;
    STAMP(0);
    const unsigned rg = ((b & 7) >> 1) * 8 + (b >> 5), cg = (b & 1) * 4 + ((b >> 3) & 3);
    const unsigned rot = rg & 3;
    const unsigned r8 = lane >> 3, c8 = lane & 7, tk = lane & 15, g = lane >> 4, q = (lane & 15) >> 2, p = lane & 3;
    const unsigned hd = w & 3, kh = w >> 2;
    const rsrc_t rx = mk(x, 2048 * 1024), rq = mk(wqkv, 512 * 3072), ro = mk(wo, 512 * 1024), r1 = mk(w1, 512 * 2752), r2 = mk(w2, 1376 * 1024);
    const rsrc_t rc = mk(cvec, 1024), rm = mk(wmod, 512 * 6144), rw = mk(ws, WS_BYTES), rout = mk(out, 2048 * 1024);
    const unsigned swz0 = (c8 ^ (r8 & 2)) * 16;
    const bool prod = b < 192;
    const unsigned grp = b >> 2, pp = b & 3;

    unsigned ep = ld4c(rw, 0, WS_EPOCH);
    unsigned cv = ld4(rc, lane * 4, pp * 256);
    {
        const unsigned so = prod ? (pp * 128 + w * 16) * 6144 + grp * 128 : 0x40000000u;
        dma1(rm, LB + L_M + w * 2048, r8 * 6144 + swz0, so);
        dma1(rm, LB + L_M + w * 2048 + 1024, r8 * 6144 + (swz0 ^ 64), so + 8 * 6144);
    }
    u32x4 xr[10];
    {
        const unsigned vo = r8 * 1024 + c8 * 16;
#pragma unroll
        for (int n = 0; n < 10; ++n) {
            int row = (int)rg * 64 - 16 + n * 8; if (row < 0) row += 16;
            xr[n] = ld16<0>(rx, vo, (unsigned)row * 1024 + w * 128);
        }
    }
    const unsigned vq = r8 * 3072 + (swz0 ^ ((w & 1) * 64));
    auto qkv_issue = [&](int r, unsigned slot) {
        const unsigned ks = kh * 8 + ((r + 2 * rot) & 7);
        dma3(rq, LB + slot + kh * 12288 + hd * 3072, vq, (ks * 32 + hd * 8) * 3072 + cg * 128);
    };
    qkv_issue(0, 96 * K); qkv_issue(1, 72 * K); qkv_issue(2, 48 * K); qkv_issue(3, 24 * K); qkv_issue(4, 0);
    const unsigned fl = ((q >> 1) & 1) | ((g & 1) << 1);
    VMW(27); TIE2(ep, cv);
    const unsigned tag = ep + 0x01010101u;
    if (w == 7) {
        const float a = lo16(cv), c = hi16(cv);
        LD(unsigned, L_SC + lane * 4) = pk2(a * __builtin_amdgcn_rcpf(1.f + __builtin_amdgcn_exp2f(-1.44269504089f * a)), c * __builtin_amdgcn_rcpf(1.f + __builtin_amdgcn_exp2f(-1.44269504089f * c)));
    }
    VMW(25); BAR();
    STAMP(1);
    const unsigned trb = (8 * g + q) * 128 + (p >> 1) * 16 + (p & 1) * 8;
    if (prod && w < 4) {
        f32x4 acc = {0, 0, 0, 0};
        const unsigned a0 = L_M + trb + ((2 * w) ^ (fl << 1)) * 16;
#pragma unroll
        for (int s = 0; s < 4; ++s) acc = mfma32(TRF(a0 + s * 4096, 512), LD(bf16x8, L_SC + s * 64 + g * 16), acc);
        u32x4 v;
#pragma unroll
        for (int j = 0; j < 4; ++j) v[j] = __builtin_amdgcn_perm(__float_as_uint(acc[j]), tag, 0x07060500u | j);
        if (tk == 0) st16c(rw, WS_MODP + (b * 64 + w * 16 + g * 4) * 4, v);
    }
#pragma unroll
    for (int h5 = 0; h5 < 2; ++h5) {
        if (h5 == 0) VMW(20); else VMW(15);
        float s[5], qq[5];
#pragma unroll
        for (int n = 0; n < 5; ++n) {
            TIE1(xr[h5 * 5 + n]);
            const u32x4 v = xr[h5 * 5 + n];
            s[n] = dot2(v[3], 0x3f803f80u, dot2(v[2], 0x3f803f80u, dot2(v[1], 0x3f803f80u, dot2(v[0], 0x3f803f80u, 0.f))));
            qq[n] = dot2(v[3], v[3], dot2(v[2], v[2], dot2(v[1], v[1], dot2(v[0], v[0], 0.f))));
        }
#define A5(F) F(0) F(1) F(2) F(3) F(4)
#define ST_A(n) "v_add_f32_dpp %" #n ", %" #n ", %" #n " row_half_mirror row_mask:0xf bank_mask:0x5\n\t"
#define ST_C(n) "v_add_f32_dpp %" #n ", %" #n ", %" #n " quad_perm:[1,0,3,2] row_mask:0xf bank_mask:0xf\n\t"
#define ST_D(n) "v_add_f32_dpp %" #n ", %" #n ", %" #n " quad_perm:[2,3,0,1] row_mask:0xf bank_mask:0xf\n\t"
#define ST_B(n, m) "v_add_f32_dpp %" #n ", %" #m ", %" #m " row_half_mirror row_mask:0xf bank_mask:0xa\n\t"
        asm volatile("s_nop 1\n\t" A5(ST_A) ST_B(0, 5) ST_B(1, 6) ST_B(2, 7) ST_B(3, 8) ST_B(4, 9) A5(ST_C) A5(ST_D)
                     : "+v"(s[0]), "+v"(s[1]), "+v"(s[2]), "+v"(s[3]), "+v"(s[4]) : "v"(qq[0]), "v"(qq[1]), "v"(qq[2]), "v"(qq[3]), "v"(qq[4]));
#pragma unroll
        for (int n = 0; n < 5; ++n) LD(float, L_STAT + ((h5 * 5 + n) * 8 + r8) * 64 + w * 8 + (c8 >> 2) * 4) = s[n];
    }
    STAMP(2);
    VMW(9); BAR();
    STAMP(3);
    qkv_issue(7, 120 * K);
    bf16x8 Wq[8][3];
    const unsigned trq = kh * 12288 + g * 3072 + q * 128 + (((2 * hd) ^ (fl << 1)) + (p >> 1)) * 16 + (p & 1) * 8;
    auto qkv_read = [&](int r, unsigned slot) {
#pragma unroll
        for (int m = 0; m < 3; ++m) Wq[r][m] = TRF(slot + trq + m * 1024, 512);
    };
    qkv_read(0, 96 * K); qkv_read(1, 72 * K);
    if (w == 4 || (w == 5 && lane < 16)) {
        const unsigned row = (w - 4) * 64 + lane;
        const f32x4 a = LD(f32x4, L_STAT + row * 64) + LD(f32x4, L_STAT + row * 64 + 16) + LD(f32x4, L_STAT + row * 64 + 32) + LD(f32x4, L_STAT + row * 64 + 48);
        const float mu = (a[0] + a[2]) * (1.f / 512.f);
        const float var = fmaxf((a[1] + a[3]) * (1.f / 512.f) - mu * mu, 0.f);
        const float rs = __builtin_amdgcn_rsqf(var + 1e-5f);
        LD(f32x2, L_RST + row * 8) = (f32x2){rs, -mu * rs};
    }
    BAR();
    STAMP(4);
    qkv_issue(5, 96 * K); qkv_issue(6, 72 * K);
    const unsigned tile0 = cg * 11 > 75 ? 75 : cg * 11;
    {
        const u32x4 ff = {~0u, ~0u, ~0u, ~0u};
        st16c(rw, WS_O + ((rg * 4 + (w >> 1)) * 16 + cg * 2 + (w & 1)) * 1024 + lane * 16, ff);
        st16c(rw, WS_Y + (rg * 64 + w * 8 + r8) * 1024 + cg * 128 + c8 * 16, ff);
        const unsigned ub = WS_U + (rg * 4 + (w >> 1)) * 44032 + tile0 * 512, uo = (w & 1) * 3072 + lane * 16;
#pragma unroll
        for (int j = 0; j < 3; ++j) { const unsigned o = uo + j * 1024; if (o < 5632 && (cg < 7 || o >= 1024)) st16c(rw, ub + o, ff); }
        if (w == 0 && lane < 32) st16c(rw, WS_ST + ((rg * 4 + r8) * 8 + cg) * 128 + c8 * 16, ff);
    }
    f32x2 t[10][4];
#pragma unroll
    for (int n = 0; n < 10; ++n) {
        const f32x2 st = LD(f32x2, L_RST + (n * 8 + r8) * 8);
#pragma unroll
        for (int j = 0; j < 4; ++j) t[n][j] = (f32x2){lo16(xr[n][j]), hi16(xr[n][j])} * st.xx + st.yy;
    }
    STAMP(5);
    VMW(13); BAR();
    STAMP(6);
    qkv_read(2, 48 * K); qkv_read(3, 24 * K); qkv_read(4, 0);
    auto mod_ld = [&](unsigned g0, unsigned g1, u32x4& a, u32x4& c) {
        a = ld16c<0>(rw, lane * 16, WS_MODP + g0 * 1024); c = ld16c<0>(rw, lane * 16, WS_MODP + g1 * 1024);
    };
    auto mod_bad = [&](u32x4 a, u32x4 c) {
        const unsigned ta = __builtin_amdgcn_perm(__builtin_amdgcn_perm(a[3], a[2], 0x0c0c0400u), __builtin_amdgcn_perm(a[1], a[0], 0x0c0c0400u), 0x05040100u);
        const unsigned tc = __builtin_amdgcn_perm(__builtin_amdgcn_perm(c[3], c[2], 0x0c0c0400u), __builtin_amdgcn_perm(c[1], c[0], 0x0c0c0400u), 0x05040100u);
        return anyl(ta != tag || tc != tag);
    };
    auto mod_fin = [&](unsigned g0, unsigned g1, u32x4 a, u32x4 c, float add1) {
        for (int it = 0; it < SPIN && mod_bad(a, c); ++it) { mod_ld(g0, g1, a, c); VMW(0); TIE2(a, c); }
        float s[4];
#pragma unroll
        for (int j = 0; j < 4; ++j) s[j] = sw16add(__uint_as_float(a[j] & 0xffffff00u), __uint_as_float(c[j] & 0xffffff00u));
        const unsigned r = pk2(sw32add(s[0], s[2]), sw32add(s[1], s[3]));
        const float ad = (g & 1) ? add1 : 0.f;
        return pk2(lo16(r) + ad, hi16(r) + ad);
    };
    u32x4 ma, mc;
    mod_ld(w, 8 + w, ma, mc);
    VMW(2); BAR();
    STAMP(7);
    if (threadIdx.x == 0) st4c(rw, WS_FLAG + (rg * 8 + cg) * 4, tag);
    qkv_read(5, 96 * K); qkv_read(6, 72 * K); qkv_read(7, 120 * K);
    VMW(0); TIE2(ma, mc);
    f32x2 A[4], S[4];
    {
        const unsigned r = mod_fin(w, 8 + w, ma, mc, 1.f);
        LD(f32x2, L_AS + w * 512 + (g & 1) * 256 + tk * 16 + (g >> 1) * 8) = (f32x2){lo16(r), hi16(r)};
        const f32x4 s0 = LD(f32x4, L_AS + w * 512 + c8 * 32), s1 = LD(f32x4, L_AS + w * 512 + c8 * 32 + 16);
        const f32x4 a0 = LD(f32x4, L_AS + w * 512 + 256 + c8 * 32), a1 = LD(f32x4, L_AS + w * 512 + 256 + c8 * 32 + 16);
        S[0] = s0.xy; S[1] = s0.zw; S[2] = s1.xy; S[3] = s1.zw; A[0] = a0.xy; A[1] = a0.zw; A[2] = a1.xy; A[3] = a1.zw;
    }
    STAMP(8);
    const unsigned vo1k0 = r8 * 1024 + swz0, vo1k1 = vo1k0 ^ 64;
    dma4(ro, LB + 32 * K + w * 4096, vo1k0, vo1k1, (w * 64) * 1024 + cg * 128);
    const unsigned wrot = kh * 4 + ((w - rot) & 3);
    const unsigned aw0 = ((2 * wrot + (c8 >> 2)) * 64 + (c8 & 3) * 16 + (r8 ^ ((c8 & 3) | ((c8 >> 2) * 12)))) * 16, aw1 = aw0 ^ 128;
    const unsigned ar0 = (kh * 512 + g * 16 + (tk ^ g)) * 16, ar1 = (kh * 512 + 64 + g * 16 + (tk ^ (g | 12))) * 16;
    auto normalize = [&](int T) {
#pragma unroll
        for (int i = 0; i < 2; ++i) {
            u32x4 h;
#pragma unroll
            for (int j = 0; j < 4; ++j) { const f32x2 v = t[T * 2 + i][j] * A[j] + S[j]; h[j] = pk2(v.x, v.y); }
            LD(u32x4, (T & 3) * 16384 + (i ? aw1 : aw0)) = h;
        }
    };
    normalize(0);
    BAR();
    STAMP(9);
    dma4(ro, LB + 64 * K + w * 4096, vo1k0, vo1k1, (w * 64 + 32) * 1024 + cg * 128);
    unsigned v1o[3];
#pragma unroll
    for (int j = 0; j < 3; ++j) { const unsigned ch = (hd + 4 * j) * 64 + lane, r = ch / 22; v1o[j] = r * 2752 + (ch - r * 22) * 16; }
    auto w1_issue = [&](int r, unsigned slot) {
#pragma unroll
        for (int h2 = 0; h2 < 2; ++h2) {
            const unsigned so = (h2 * 8 + ((r + 2 * rot) & 7)) * (32 * 2752) + tile0 * 32, m0 = LB + slot + h2 * 11264 + hd * 1024;
            dma1(r1, m0, v1o[0], so); dma1(r1, m0 + 4096, v1o[1], so);
            dma1(r1, m0 + (hd < 3 ? 8192 : 4096), hd < 3 ? v1o[2] : v1o[1], so);
        }
    };
    if (kh == 0) w1_issue(0, 96 * K); else w1_issue(1, 118 * K);
    bf16x8 Wo[2][4], W1[8][3], W2[6][4];
    const unsigned tre = trb + (fl << 1) * 16, tro = trb + (2 ^ (fl << 1)) * 16;
    auto row_read = [&](bf16x8* W, unsigned base) {
        W[0] = TRF(base + tre, 512); W[1] = TRF(base + tro, 512); W[2] = TRF(base + (tre ^ 64), 512); W[3] = TRF(base + (tro ^ 64), 512);
    };
    const unsigned tr1 = kh * 11264 + (4 * g + q) * 352 + hd * 96 + p * 8;
    auto w1_read = [&](int r, unsigned slot) {
#pragma unroll
        for (int m = 0; m < 3; ++m) W1[r][m] = TRF(slot + tr1 + m * 32, 5632);
    };

    const unsigned xp = L_XCH + hd * 4096 + lane * 16;
    u32x4 kv = {0, 0, 0, 0};
    u32x4 m2a, m2c, m3a, m3c, of[4]; u32x2 xres[2];
    const unsigned fb = (lane >> 3) & 1, fg = (lane >> 1) & 3, fe = lane & 1, ftok = hd * 4 + g;
    const unsigned fcol = cg * 128 + fb * 64 + fe * 32 + fg * 8;
    const unsigned cbw = (g & 1) * 1024 + (tk >> 3) * 64 + (tk & 3) * 16 + ((tk >> 2) & 1) * 8 + (g >> 1) * 4;
#pragma unroll
    for (int T = 0; T < 5; ++T) {
        const bool fin = kh == (unsigned)(T & 1);
        if (T == 2 && fin) { w1_issue(2, 74 * K); mod_ld(24 + w, 32 + w, m2a, m2c); }
        if (T == 3 && fin) { w1_issue(3, 16 * K); mod_ld(24 + w, 32 + w, m2a, m2c); mod_ld(16 + cg, 40 + cg, m3a, m3c); }
        if (T == 4 && fin) { w1_issue(4, 96 * K); w1_issue(5, 118 * K); }
        f32x4 aq = {0, 0, 0, 0}, ak = {0, 0, 0, 0}, av = {0, 0, 0, 0};
#pragma unroll
        for (int s = 0; s < 8; ++s) {
            const bf16x8 hf = LD(bf16x8, (T & 3) * 16384 + (s >> 1) * 2048 + ((s & 1) ? ar1 : ar0));
            if (T > 0) aq = mfma32(Wq[s][0], hf, aq);
            ak = mfma32(Wq[s][1], hf, ak);
            av = mfma32(hf, Wq[s][2], av);
        }
        if (T == 0) { normalize(1); VMW(10); row_read(Wo[0], 32 * K + w * 4096); }
        if (T == 1) { normalize(2); normalize(3); VMW(6); row_read(Wo[1], 64 * K + w * 4096); }
        if (T == 2) normalize(4);
        if (T == 3) { w1_read(0, 96 * K); w1_read(1, 118 * K); }
        if (T == 4) { w1_read(2, 74 * K); w1_read(3, 16 * K); }
        if (!fin) {
            if (T > 0) { LD(f32x4, xp) = aq; LD(u32x4, xp + 3072) = kv; }
            LD(f32x4, xp + 1024) = ak; LD(f32x4, xp + 2048) = av;
            if (T == 2 || T == 3) VMW(1);
        } else {
            if (T == 2 || T == 3) {
                VMW(0);
                if (T == 3) { TIE2(m3a, m3c); if (w == 7) LD(unsigned, L_GT + cbw - (g & 1) * 896) = mod_fin(16 + cg, 40 + cg, m3a, m3c, 0.f); }
                TIE2(m2a, m2c);
                LD(unsigned, L_CB + wrot * 128 + cbw) = mod_fin(24 + w, 32 + w, m2a, m2c, 1.f);
            }
            if (T == 4) {
                if (w == 0) {
                    for (int it = 0; it < SPIN; ++it) { unsigned f = ld4c(rw, c8 * 4, WS_FLAG + rg * 32); VMW(0); TIE1(f); if (!anyl(f != tag)) break; }
                }
                VMW(0);
            }
        }
        BAR();
        STAMP(10 + T);
        if (fin) {
            ak += LD(f32x4, xp + 1024); av += LD(f32x4, xp + 2048);
            kv = (u32x4){pk2(ak[0], ak[1]), pk2(ak[2], ak[3]), pk2(av[0], av[1]), pk2(av[2], av[3])};
            if (T > 0) {
                aq += LD(f32x4, xp);
                const u32x4 kvp = LD(u32x4, xp + 3072);
                const float NI = -__builtin_inff();
                f32x4 mcur, mprev; unsigned M[4];
                {
                    bool vc[4], vp[4];
#pragma unroll
                    for (int r = 0; r < 4; ++r) {
                        const int d = (int)tk - (int)(4 * g + r); vc[r] = d >= 0 && !(d & 1); vp[r] = d <= -2 && !(d & 1);
                        mcur[r] = vc[r] ? 0.f : NI; mprev[r] = vp[r] ? 0.f : NI;
                    }
                    M[0] = (vp[0] ? 0xffffu : 0u) | (vp[1] ? 0xffff0000u : 0u); M[1] = (vp[2] ? 0xffffu : 0u) | (vp[3] ? 0xffff0000u : 0u);
                    M[2] = (vc[0] ? 0xffffu : 0u) | (vc[1] ? 0xffff0000u : 0u); M[3] = (vc[2] ? 0xffffu : 0u) | (vc[3] ? 0xffff0000u : 0u);
                }
                if (T == 1 && rg == 0) mprev = (f32x4){NI, NI, NI, NI};
                const unsigned q0 = pk2(aq[0], aq[1]), q1 = pk2(aq[2], aq[3]);
                const f32x4 sp = mfma16u(kvp[0], kvp[1], q0, q1, mprev), sc = mfma16u(kv[0], kv[1], q0, q1, mcur);
                const float t0 = fmx(fmx(fmx(sp[0], sp[1]), sc[0]), sc[1]), t1 = fmx(fmx(fmx(sp[2], sp[3]), sc[2]), sc[3]);
                const float C = 0.25f * 1.44269504089f;
                const float mx = xmax32(xmax16(fmx(t0, t1))) * C;
                const float e0 = __builtin_amdgcn_exp2f(__builtin_fmaf(t0, C, -mx)), e1 = __builtin_amdgcn_exp2f(__builtin_fmaf(t1, C, -mx));
                const unsigned p0 = pk2(e0, e0), p1 = pk2(e1, e1);
                const u32x4 P = {p0 & M[0], p1 & M[1], p0 & M[2], p1 & M[3]};
                const f32x4 o = mfma32u((u32x4){kvp[2], kvp[3], kv[2], kv[3]}, P, (f32x4){0, 0, 0, 0});
                const f32x4 sm = mfma32u((u32x4){0x3f803f80u, 0x3f803f80u, 0x3f803f80u, 0x3f803f80u}, P, (f32x4){0, 0, 0, 0});
                const float inv = __builtin_amdgcn_rcpf(sm[0]);
                const unsigned oa = nosent(pk2(o[0] * inv, o[1] * inv)), ob = nosent(pk2(o[2] * inv, o[3] * inv));
                auto ra = __builtin_amdgcn_permlane16_swap(oa, oa, false, false);
                auto rb = __builtin_amdgcn_permlane16_swap(ob, ob, false, false);
                if (!(g & 1)) st16c(rw, WS_O + (((rg * 4 + T - 1) * 16 + cg * 2 + (hd >> 1)) * 64 + ((hd & 1) * 2 + (g >> 1)) * 16 + tk) * 16, (u32x4){ra[0], rb[0], ra[1], rb[1]});
            }
        }
    }
    STAMP(15);

    const unsigned pw0 = w * 4096 + tk * 256 + (g ^ (tk & 7)) * 16, pw1 = pw0 ^ 64;
    const unsigned pr = ftok * 256 + ((fb * 8 + fe * 4 + fg) ^ (ftok & 7)) * 16;
    const unsigned ks0 = w * 5 + (w < 3 ? w : 3);
    auto w2_issue = [&](int s, unsigned slot) { dma4(r2, LB + slot + w * 4096, vo1k0, vo1k1, (ks0 + s) * 32768 + cg * 128); };
    u32x2 x1p[2];
    f32x4 ga, A2;
#pragma unroll
    for (int hf = 0; hf < 2; ++hf) {
        const unsigned so = WS_O + ((rg * 4 + hf * 2) * 16 + 2 * w) * 1024;
        auto o_ld = [&]() { of[0] = ld16c<0>(rw, lane * 16, so); of[1] = ld16c<1024>(rw, lane * 16, so); of[2] = ld16c<0>(rw, lane * 16, so + 16384); of[3] = ld16c<1024>(rw, lane * 16, so + 16384); };
        o_ld();
        xres[hf] = ld8(rx, ftok * 1024 + fcol, (rg * 64 + hf * 32 + kh * 16) * 1024);
        if (hf == 0) {
            if (kh == 1) w1_issue(6, 74 * K);
            w1_read(4, 96 * K); w1_read(5, 118 * K);
            if (kh == 1) VMW(6); else VMW(0);
        } else {
            VMW(0);
        }
        TIE2(of[0], of[1]); TIE2(of[2], of[3]); TIE1(xres[hf]);
        for (int it = 0; it < SPIN && anyl(umax4(of[3], umax4(of[2], umax4(of[1], umax4(of[0], 0)))) == ~0u); ++it) { o_ld(); VMW(0); TIE2(of[0], of[1]); TIE2(of[2], of[3]); }
        STAMP(16 + hf * 4);
#pragma unroll
        for (int tt = 0; tt < 2; ++tt)
#pragma unroll
            for (int c = 0; c < 4; ++c) {
                f32x4 acc = mfma32(Wo[0][c], __builtin_bit_cast(bf16x8, of[tt * 2]), (f32x4){0, 0, 0, 0});
                acc = mfma32(Wo[1][c], __builtin_bit_cast(bf16x8, of[tt * 2 + 1]), acc);
                LD(f32x4, tt * 32768 + (c >> 1) * 128 + ((c & 1) ? pw1 : pw0)) = acc;
            }
        BAR();
        STAMP(17 + hf * 4);
        if (hf == 0) {
            w2_issue(0, L_W2A);
            if (kh == 0) w1_issue(7, 96 * K);
            const u32x2 gv = LD(u32x2, L_GT + fb * 64 + fg * 16 + fe * 8);
            const u32x2 av = LD(u32x2, L_CB + 1024 + ((cg & 4) + ((cg - rot) & 3)) * 128 + fb * 64 + fg * 16 + fe * 8);
            ga = (f32x4){lo16(gv[0]), hi16(gv[0]), lo16(gv[1]), hi16(gv[1])};
            A2 = (f32x4){lo16(av[0]), hi16(av[0]), lo16(av[1]), hi16(av[1])};
        }
        {
            f32x4 sum = LD(f32x4, kh * 32768 + pr);
#pragma unroll
            for (int ww = 1; ww < 8; ++ww) sum += LD(f32x4, kh * 32768 + ww * 4096 + pr);
            const f32x4 xv = {lo16(xres[hf][0]), hi16(xres[hf][0]), lo16(xres[hf][1]), hi16(xres[hf][1])};
            const f32x4 v = xv + ga * sum;
            x1p[hf] = (u32x2){pk2(v[0], v[1]), pk2(v[2], v[3])};
            const f32x4 xx = (f32x4){lo16(x1p[hf][0]), hi16(x1p[hf][0]), lo16(x1p[hf][1]), hi16(x1p[hf][1])} * A2;
            const unsigned y0 = nosent(pk2(xx[0], xx[1])), y1 = nosent(pk2(xx[2], xx[3]));
            const unsigned tile = rg * 4 + hf * 2 + kh;
            const u32x4 yy = {y0, y1, dppu<0xF5>(y0), dppu<0xF5>(y1)};
            if (fe == 0) st16c(rw, WS_Y + (tile * 16 + ftok) * 1024 + cg * 128 + tk * 8, yy);
            float s = dot2(x1p[hf][1], 0x3f803f80u, dot2(x1p[hf][0], 0x3f803f80u, 0.f));
            const float qq = dot2(x1p[hf][1], x1p[hf][1], dot2(x1p[hf][0], x1p[hf][0], 0.f));
            asm volatile("s_nop 1\n\tv_add_f32_dpp %0, %0, %0 row_ror:8 row_mask:0xf bank_mask:0x3\n\tv_add_f32_dpp %0, %1, %1 row_ror:8 row_mask:0xf bank_mask:0xc\n\ts_nop 1" : "+v"(s) : "v"(qq));
            s += dpp<0x141>(s); s += dpp<0xB1>(s); s += dpp<0x4E>(s);
            if (c8 == 0) st4c(rw, WS_ST + (tile * 8 + cg) * 128 + ftok * 8 + fb * 4, nosent(__float_as_uint(s)));
        }
        STAMP(18 + hf * 4);
        if (hf == 0) {
            VMW(2);
            BAR();
            w1_read(6, 74 * K); w1_read(7, 96 * K);
            row_read(W2[0], L_W2A + w * 4096);
            asm volatile("s_waitcnt lgkmcnt(0)" ::: "memory");
            w2_issue(1, L_W2A);
            f32x4 acc[3] = {{0, 0, 0, 0}, {0, 0, 0, 0}, {0, 0, 0, 0}};
#pragma unroll
            for (int s = 0; s < 8; ++s) {
                const bf16x8 yf = LD(bf16x8, L_CB + (tk & 1) * 1024 + (kh * 8 + s) * 64 + g * 16);
#pragma unroll
                for (int m = 0; m < 3; ++m) acc[m] = mfma32(W1[s][m], yf, acc[m]);
            }
            if (tk < 2) {
#pragma unroll
                for (int m = 0; m < 3; ++m) LD(f32x4, L_S1P + kh * 1536 + tk * 768 + (hd * 48 + m * 16 + g * 4) * 4) = acc[m];
            }
            STAMP(19);
        }
    }
    row_read(W2[1], L_W2A + w * 4096);
    if (threadIdx.x < 96) LD(f32x4, L_S1 + threadIdx.x * 16) = LD(f32x4, L_S1P + threadIdx.x * 16) + LD(f32x4, L_S1P + 1536 + threadIdx.x * 16);

    u32x4 yv[2], sv;
    const unsigned vy = r8 * 1024 + c8 * 16;
    auto y_ld = [&](int tt) {
        const unsigned so = WS_Y + (rg * 4 + tt) * 16384 + w * 128;
        yv[0] = ld16c<0>(rw, vy, so); yv[1] = ld16c<0>(rw, vy, so + 8192);
        sv = ld16c<0>(rw, lane * 16, (w == (unsigned)(((tt + 1) & 1) * 4)) ? WS_ST + (rg * 4 + tt) * 1024 : 0x40000000u);
    };
    auto y_put = [&](int tt) {
        TIE2(yv[0], yv[1]); TIE1(sv);
        for (int it = 0; it < SPIN && anyl(umax4(sv, umax4(yv[1], umax4(yv[0], 0))) == ~0u); ++it) { y_ld(tt); VMW(0); TIE2(yv[0], yv[1]); TIE1(sv); }
        LD(u32x4, L_ACT + (tt & 1) * 16384 + aw0) = yv[0]; LD(u32x4, L_ACT + (tt & 1) * 16384 + aw1) = yv[1];
        if (w == (unsigned)(((tt + 1) & 1) * 4)) {
            float s = sw16add(__uint_as_float(sv[0]), __uint_as_float(sv[2])), qq = sw16add(__uint_as_float(sv[1]), __uint_as_float(sv[3]));
            s += dpp<0x128>(s); qq += dpp<0x128>(qq);
            s = sw32add(s, s); qq = sw32add(qq, qq);
            const float mu = s * (1.f / 512.f);
            const float var = fmaxf(qq * (1.f / 512.f) - mu * mu, 0.f);
            LD(f32x2, L_SST + tt * 128 + (c8 * 2 + (g & 1)) * 8) = (f32x2){-mu, __builtin_amdgcn_rsqf(var + 1e-5f)};
        }
    };
    y_ld(0);
    asm volatile("s_waitcnt lgkmcnt(0)" ::: "memory");
    w2_issue(2, L_W2A);
    VMW(4);
    y_put(0);
    BAR();
    STAMP(24);
    const unsigned xp1 = L_XC1 + hd * 3072 + lane * 16;
    u32x4 uf[6];
    auto u_ld = [&](int tt) {
        const unsigned so = WS_U + ((rg * 4 + tt) * 43 + ks0) * 1024;
        uf[0] = ld16c<0>(rw, lane * 16, so); uf[1] = ld16c<1024>(rw, lane * 16, so); uf[2] = ld16c<2048>(rw, lane * 16, so); uf[3] = ld16c<3072>(rw, lane * 16, so);
        uf[4] = ld16c<0>(rw, lane * 16, so + 4096); uf[5] = ld16c<0>(rw, lane * 16, w < 3 ? so + 5120 : 0x40000000u);
    };
#pragma unroll
    for (int tt = 0; tt < 4; ++tt) {
        const bool fin = kh == (unsigned)(tt & 1);
        if (tt < 3) y_ld(tt + 1);
        if (tt == 0) { w2_issue(3, 0); w2_issue(4, 32 * K); }
        if (tt == 1) w2_issue(5, L_W2A);
        f32x4 acc[3] = {{0, 0, 0, 0}, {0, 0, 0, 0}, {0, 0, 0, 0}};
#pragma unroll
        for (int s = 0; s < 8; ++s) {
            const bf16x8 yf = LD(bf16x8, L_ACT + (tt & 1) * 16384 + (s >> 1) * 2048 + ((s & 1) ? ar1 : ar0));
#pragma unroll
            for (int m = 0; m < 3; ++m) acc[m] = mfma32(W1[s][m], yf, acc[m]);
        }
        if (!fin) {
#pragma unroll
            for (int m = 0; m < 3; ++m) LD(f32x4, xp1 + m * 1024) = acc[m];
        }
        if (tt == 0) { VMW(8); row_read(W2[2], L_W2A + w * 4096); }
        if (tt == 1) { VMW(4); row_read(W2[3], w * 4096); row_read(W2[4], 32 * K + w * 4096); }
        if (tt == 2) VMW(0);
        if (tt == 3) row_read(W2[5], L_W2A + w * 4096);
        if (tt < 3) y_put(tt + 1);
        BAR();
        STAMP(25 + tt);
        if (fin) {
            const f32x2 st = LD(f32x2, L_SST + tt * 128 + tk * 8);
            unsigned ua[3], ub[3];
#pragma unroll
            for (int m = 0; m < 3; ++m) {
                const f32x4 t1 = LD(f32x4, L_S1 + (hd * 48 + m * 16 + g * 4) * 4), s1 = LD(f32x4, L_S1 + 768 + (hd * 48 + m * 16 + g * 4) * 4);
                const f32x4 a = acc[m] + LD(f32x4, xp1 + m * 1024);
                float v[4];
#pragma unroll
                for (int r = 0; r < 4; ++r) {
                    const float z = __builtin_fmaf(st.y, __builtin_fmaf(st.x, s1[r], a[r]), t1[r]);
                    v[r] = z * __builtin_amdgcn_rcpf(1.f + __builtin_amdgcn_exp2f(-1.44269504089f * z));
                }
                ua[m] = nosent(pk2(v[0], v[1])); ub[m] = nosent(pk2(v[2], v[3]));
            }
            const unsigned ubase = WS_U + (rg * 4 + tt) * 44032 + (tile0 + hd * 3) * 512 + (g >> 1) * 256 + tk * 16;
            {
                auto ra = __builtin_amdgcn_permlane16_swap(ua[0], ua[1], false, false);
                auto rb = __builtin_amdgcn_permlane16_swap(ub[0], ub[1], false, false);
                if (cg < 7 || hd > 0) st16c(rw, ubase + (g & 1) * 512, (u32x4){ra[0], rb[0], ra[1], rb[1]});
            }
            if (hd < 3) {
                auto ra = __builtin_amdgcn_permlane16_swap(ua[2], ua[2], false, false);
                auto rb = __builtin_amdgcn_permlane16_swap(ub[2], ub[2], false, false);
                if (!(g & 1)) st16c(rw, ubase + 1024, (u32x4){ra[0], rb[0], ra[1], rb[1]});
            }
        }
    }
    STAMP(29);

    f32x4 gb;
    {
        const u32x2 gv = LD(u32x2, L_GT + 128 + fb * 64 + fg * 16 + fe * 8);
        gb = (f32x4){lo16(gv[0]), hi16(gv[0]), lo16(gv[1]), hi16(gv[1])};
    }
    u_ld(0);
#pragma unroll
    for (int tt = 0; tt < 4; ++tt) {
        VMW(0);
        TIE2(uf[0], uf[1]); TIE2(uf[2], uf[3]); TIE2(uf[4], uf[5]);
        for (int it = 0; it < SPIN && anyl(umax4(uf[5], umax4(uf[4], umax4(uf[3], umax4(uf[2], umax4(uf[1], umax4(uf[0], 0)))))) == ~0u); ++it) {
            u_ld(tt); VMW(0); TIE2(uf[0], uf[1]); TIE2(uf[2], uf[3]); TIE2(uf[4], uf[5]);
        }
        STAMP(30 + tt * 2);
        u32x4 uc[6];
#pragma unroll
        for (int s = 0; s < 6; ++s) uc[s] = uf[s];
        if (tt < 3) u_ld(tt + 1);
        f32x4 acc[4] = {{0, 0, 0, 0}, {0, 0, 0, 0}, {0, 0, 0, 0}, {0, 0, 0, 0}};
#pragma unroll
        for (int s = 0; s < 5; ++s)
#pragma unroll
            for (int c = 0; c < 4; ++c) acc[c] = mfma32(W2[s][c], __builtin_bit_cast(bf16x8, uc[s]), acc[c]);
        if (w < 3) {
#pragma unroll
            for (int c = 0; c < 4; ++c) acc[c] = mfma32(W2[5][c], __builtin_bit_cast(bf16x8, uc[5]), acc[c]);
        }
#pragma unroll
        for (int c = 0; c < 4; ++c) LD(f32x4, (tt & 1) * 32768 + (c >> 1) * 128 + ((c & 1) ? pw1 : pw0)) = acc[c];
        BAR();
        STAMP(31 + tt * 2);
        if (kh == (unsigned)(tt & 1)) {
            f32x4 sum = LD(f32x4, kh * 32768 + pr);
#pragma unroll
            for (int ww = 1; ww < 8; ++ww) sum += LD(f32x4, kh * 32768 + ww * 4096 + pr);
            const u32x2 xp2 = x1p[tt >> 1];
            const f32x4 v = (f32x4){lo16(xp2[0]), hi16(xp2[0]), lo16(xp2[1]), hi16(xp2[1])} + gb * sum;
            __builtin_amdgcn_raw_buffer_store_b64((u32x2){pk2(v[0], v[1]), pk2(v[2], v[3])}, rout, (int)(((rg * 4 + tt) * 16 + ftok) * 1024 + fcol), 0, 0);
        }
    }
    if (b == 0 && w == 0) {
        for (int it = 0; it < SPIN; ++it) {
            u32x4 f = ld16c<0>(rw, lane * 16, WS_FLAG); VMW(0); TIE1(f);
            if (!anyl(f[0] != tag || f[1] != tag || f[2] != tag || f[3] != tag)) break;
        }
        if (lane == 0) st4c(rw, WS_EPOCH, tag);
    }
    STAMP(38);
}

extern "C" void kernel_launch(void* const* d_in, const int* in_sizes, int n_in, void* d_out, int out_size, void* d_ws, size_t ws_size, hipStream_t stream) {
    fused_block<<<256, 512, 0, stream>>>((const char*)d_in[0], (const char*)d_in[1], (const char*)d_in[2], (const char*)d_in[4], (const char*)d_in[5],
                                         (const char*)d_in[6], (const char*)d_in[7], (char*)d_ws, (char*)d_out);
}
```

```cpp
#include <hip/hip_runtime.h>
#include <stdint.h>

#if defined(__HIP_DEVICE_COMPILE__) && !defined(PK)
#pragma clang attribute push(__attribute__((target("no-packed-fp32-ops"))), apply_to = function)
#endif
#define LAS __attribute__((address_space(3)))
typedef __bf16 bf16x8 __attribute__((ext_vector_type(8)));
typedef __bf16 bf16x4 __attribute__((ext_vector_type(4)));
typedef __bf16 bf16x2 __attribute__((ext_vector_type(2)));
typedef short s16x4 __attribute__((ext_vector_type(4)));
typedef float f32x4 __attribute__((ext_vector_type(4)));
typedef float f32x2 __attribute__((ext_vector_type(2)));
typedef unsigned u32x4 __attribute__((ext_vector_type(4)));
typedef unsigned u32x2 __attribute__((ext_vector_type(2)));
typedef __amdgpu_buffer_rsrc_t rsrc_t;
#define DI __device__ __forceinline__
#define U2F(x) __builtin_bit_cast(float, (unsigned)(x))
#define F2U(x) __builtin_bit_cast(unsigned, (float)(x))
#define MAP 1
#define ROT 1

constexpr unsigned WS_EPOCH = 0, WS_FLAG = 1024, WS_MODP = 4096, WS_ST = 65536, WS_O = 1u << 20, WS_Y = 3u << 20, WS_U = 5u << 20, WS_BYTES = 11u << 20;
constexpr int SPIN = 1 << 15;
constexpr unsigned K = 1024;
constexpr unsigned L_M = 120 * K, L_STAT = 144 * K, L_RST = 149 * K, L_SC = 150 * K, L_AS = 152 * K, L_XCH = 140 * K, L_MK = 156 * K;
constexpr unsigned L_CB = 64 * K, L_GT = 66 * K, L_SST = 67 * K, L_S1P = 68 * K, L_S1 = 72 * K;
constexpr unsigned L_ACT = 74 * K, L_XC1 = 106 * K, L_W2A = 128 * K;

DI unsigned pk2(float a, float b) { bf16x2 v = {(__bf16)a, (__bf16)b}; return __builtin_bit_cast(unsigned, v); }
DI float lo16(unsigned u) { return U2F(u << 16); }
DI float hi16(unsigned u) { return U2F(u & 0xffff0000u); }
template <int CTRL> DI float dpp(float v) { return U2F(__builtin_amdgcn_update_dpp(0, (int)F2U(v), CTRL, 0xf, 0xf, true)); }
template <int CTRL> DI unsigned dppu(unsigned v) { return (unsigned)__builtin_amdgcn_update_dpp(0, (int)v, CTRL, 0xf, 0xf, true); }
DI float fmx(float a, float b) { return __builtin_elementwise_maximum(a, b); }
DI float dot2(unsigned a, unsigned b, float c) { return __builtin_amdgcn_fdot2_f32_bf16(__builtin_bit_cast(bf16x2, a), __builtin_bit_cast(bf16x2, b), c, false); }
DI float sw16add(float a, float b) { auto r = __builtin_amdgcn_permlane16_swap(F2U(a), F2U(b), false, false); return U2F(r[0]) + U2F(r[1]); }
DI float sw32add(float a, float b) { auto r = __builtin_amdgcn_permlane32_swap(F2U(a), F2U(b), false, false); return U2F(r[0]) + U2F(r[1]); }
DI float xmax16(float v) { auto r = __builtin_amdgcn_permlane16_swap(F2U(v), F2U(v), false, false); return fmx(U2F(r[0]), U2F(r[1])); }
DI float xmax32(float v) { auto r = __builtin_amdgcn_permlane32_swap(F2U(v), F2U(v), false, false); return fmx(U2F(r[0]), U2F(r[1])); }
DI f32x4 mfma32(bf16x8 a, bf16x8 b, f32x4 c) { return __builtin_amdgcn_mfma_f32_16x16x32_bf16(a, b, c, 0, 0, 0); }
DI f32x4 mfma32u(u32x4 a, u32x4 b, f32x4 c) { return mfma32(__builtin_bit_cast(bf16x8, a), __builtin_bit_cast(bf16x8, b), c); }
DI f32x4 mfma16u(unsigned a0, unsigned a1, unsigned b0, unsigned b1, f32x4 c) {
    return __builtin_amdgcn_mfma_f32_16x16x16bf16_1k(__builtin_bit_cast(s16x4, (u32x2){a0, a1}), __builtin_bit_cast(s16x4, (u32x2){b0, b1}), c, 0, 0, 0);
}
DI rsrc_t mk(const void* p, unsigned n) { return __builtin_amdgcn_make_buffer_rsrc((void*)p, (short)0, (int)n, 0x00020000); }
DI unsigned nosent(unsigned v) { return v < 0xfffffffeu ? v : 0xfffffffeu; }
DI unsigned umax3(unsigned a, unsigned b, unsigned c) { unsigned m = a > b ? a : b; return m > c ? m : c; }
DI unsigned umax4(u32x4 v, unsigned m) { return umax3(umax3(v[0], v[1], m), v[2], v[3]); }

#define BAR() asm volatile("s_waitcnt lgkmcnt(0)\n\ts_barrier" ::: "memory")
#define VMW(n) asm volatile("s_waitcnt vmcnt(%0)" ::"n"(n) : "memory")
#define TIE1(a) asm volatile("; TIE %0" : "+v"(a))
#define TIE2(a, b) asm volatile("; TIE %0 %1" : "+v"(a), "+v"(b))
DI void dma1(rsrc_t r, unsigned m0, unsigned vo, unsigned so) {
    asm volatile("s_mov_b32 m0, %0\n\ts_nop 0\n\tbuffer_load_dwordx4 %1, %2, %3 offen lds" ::"s"(m0), "v"(vo), "s"(r), "s"(so) : "m0", "memory");
}
DI void dma3(rsrc_t r, unsigned m0, unsigned vo, unsigned so) {
    asm volatile("s_mov_b32 m0, %0\n\ts_nop 0\n\tbuffer_load_dwordx4 %1, %2, %3 offen lds\n\tbuffer_load_dwordx4 %1, %2, %3 offen offset:1024 lds\n\tbuffer_load_dwordx4 %1, %2, %3 offen offset:2048 lds"
                 ::"s"(m0), "v"(vo), "s"(r), "s"(so) : "m0", "memory");
}
DI void dma4(rsrc_t r, unsigned m0, unsigned vo0, unsigned vo1, unsigned so) {
    asm volatile("s_mov_b32 m0, %0\n\ts_nop 0\n\tbuffer_load_dwordx4 %1, %3, %4 offen lds\n\tbuffer_load_dwordx4 %2, %3, %5 offen offset:1024 lds\n\t"
                 "buffer_load_dwordx4 %1, %3, %6 offen offset:2048 lds\n\tbuffer_load_dwordx4 %2, %3, %7 offen offset:3072 lds"
                 ::"s"(m0), "v"(vo0), "v"(vo1), "s"(r), "s"(so), "s"(so + 7168u), "s"(so + 14336u), "s"(so + 21504u) : "m0", "memory");
}
template <int OFF> DI u32x4 ld16(rsrc_t r, unsigned vo, unsigned so) {
    u32x4 v; asm volatile("buffer_load_dwordx4 %0, %1, %2, %3 offen offset:%4" : "=v"(v) : "v"(vo), "s"(r), "s"(so), "n"(OFF) : "memory"); return v;
}
template <int OFF> DI u32x4 ld16c(rsrc_t r, unsigned vo, unsigned so) {
    u32x4 v; asm volatile("buffer_load_dwordx4 %0, %1, %2, %3 offen offset:%4 sc1" : "=v"(v) : "v"(vo), "s"(r), "s"(so), "n"(OFF) : "memory"); return v;
}
DI u32x2 ld8(rsrc_t r, unsigned vo, unsigned so) {
    u32x2 v; asm volatile("buffer_load_dwordx2 %0, %1, %2, %3 offen" : "=v"(v) : "v"(vo), "s"(r), "s"(so) : "memory"); return v;
}
DI unsigned ld4(rsrc_t r, unsigned vo, unsigned so) {
    unsigned v; asm volatile("buffer_load_dword %0, %1, %2, %3 offen" : "=v"(v) : "v"(vo), "s"(r), "s"(so) : "memory"); return v;
}
DI unsigned ld4c(rsrc_t r, unsigned vo, unsigned so) {
    unsigned v; asm volatile("buffer_load_dword %0, %1, %2, %3 offen sc1" : "=v"(v) : "v"(vo), "s"(r), "s"(so) : "memory"); return v;
}
DI void st16c(rsrc_t r, unsigned vo, u32x4 v) { __builtin_amdgcn_raw_buffer_store_b128(v, r, (int)vo, 0, 16); }
DI void st4c(rsrc_t r, unsigned vo, unsigned v) { __builtin_amdgcn_raw_buffer_store_b32(v, r, (int)vo, 0, 16); }
DI void st16s(rsrc_t r, unsigned vo, unsigned so, u32x4 v) { __builtin_amdgcn_raw_buffer_store_b128(v, r, (int)vo, (int)so, 16); asm volatile("s_nop 1" ::"v"(v) : "memory"); }
DI void st4s(rsrc_t r, unsigned vo, unsigned so, unsigned v) { __builtin_amdgcn_raw_buffer_store_b32(v, r, (int)vo, (int)so, 16); }
#define SB() __builtin_amdgcn_sched_barrier(0)
#define PIN(a) asm volatile("" : "+v"(a))
#define UNLIKELY(c) __builtin_expect(!!(c), 0)
constexpr unsigned OOB = 0x40000000u;
DI unsigned newlane() { unsigned l; asm volatile("v_mbcnt_lo_u32_b32 %0, -1, 0\n\tv_mbcnt_hi_u32_b32 %0, -1, %0" : "=v"(l)); return l; }
DI bool anyl(bool c) { return __builtin_amdgcn_ballot_w64(c) != 0; }

#define STAMP(i)
#define STAMPC(i)

DI void dma4s(rsrc_t r, unsigned m0, unsigned vo, unsigned so, unsigned stride) {
    asm volatile("s_mov_b32 m0, %0\n\ts_nop 0\n\tbuffer_load_dwordx4 %1, %2, %3 offen lds\n\tbuffer_load_dwordx4 %1, %2, %4 offen offset:1024 lds\n\t"
                 "buffer_load_dwordx4 %1, %2, %5 offen offset:2048 lds\n\tbuffer_load_dwordx4 %1, %2, %6 offen offset:3072 lds"
                 ::"s"(m0), "v"(vo), "s"(r), "s"(so), "s"(so + stride - 1024u), "s"(so + 2 * (stride - 1024u)), "s"(so + 3 * (stride - 1024u)) : "m0", "memory");
}

__global__ __launch_bounds__(512) void fused_block(const char* x, const char* wqkv, const char* wo, const char* w1, const char* w2,
                                                   const char* cvec, const char* wmod, char* ws, char* out) {
    __shared__ __attribute__((aligned(16))) char lds[163840];
    LAS char* L = (LAS char*)lds;
    const unsigned LB = (unsigned)(uintptr_t)L;
#define LD(T, off) (*(LAS T*)(L + (off)))
#define TRF(off, d) __builtin_shufflevector(__builtin_amdgcn_ds_read_tr16_b64_v4bf16((LAS bf16x4*)(L + (off))), __builtin_amdgcn_ds_read_tr16_b64_v4bf16((LAS bf16x4*)(L + (off) + (d))), 0, 1, 2, 3, 4, 5, 6, 7)
    unsigned lane = __builtin_amdgcn_workitem_id_x() & 63; const unsigned w = __builtin_amdgcn_readfirstlane(__builtin_amdgcn_workitem_id_x() >> 6);
    const unsigned b = __builtin_amdgcn_workgroup_id_x();
    STAMP(0); STAMPC(62);
    const unsigned rg = ((b & 7) >> 1) * 8 + (b >> 5), cg = (b & 1) * 4 + ((b >> 3) & 3);
    const unsigned rot = rg & 3;
    unsigned r8, c8, tk, g, q, p, l16, vy, swz0, fl, vo1k0, vo1k1, tre, tro, vos;
    auto derive = [&]() {
        r8 = lane >> 3; c8 = lane & 7; tk = lane & 15; g = lane >> 4; q = (lane & 15) >> 2; p = lane & 3;
        l16 = lane * 16; vy = r8 * 1024 + c8 * 16; swz0 = (c8 ^ (r8 & 2)) * 16; fl = ((q >> 1) & 1) | ((g & 1) << 1);
        vo1k0 = r8 * 1024 + swz0; vo1k1 = vo1k0 ^ 64;
        const unsigned trb = (8 * g + q) * 128 + (p >> 1) * 16 + (p & 1) * 8 + w * 4096;
        tre = trb + (fl << 1) * 16; tro = trb + (2 ^ (fl << 1)) * 16;
        vos = (g & 1) ? OOB : (g >> 1) * 256 + tk * 16;
    };
    derive();
#define RELANE() do { lane = newlane(); derive(); } while (0)
    const unsigned hd = w & 3, kh = w >> 2;
    const rsrc_t rx = mk(x, 2048 * 1024), rq = mk(wqkv, 512 * 3072), ro = mk(wo, 512 * 1024), r1 = mk(w1, 512 * 2752), r2 = mk(w2, 1376 * 1024);
    const rsrc_t rc = mk(cvec, 1024), rm = mk(wmod, 512 * 6144), rw = mk(ws, WS_BYTES), rout = mk(out, 2048 * 1024);
    const bool prod = b < 192 && w < 4;

    unsigned ep = ld4c(rw, 0, WS_EPOCH);
    unsigned cv;
    if (prod) {
        cv = ld4(rc, lane * 4, (b & 3) * 256);
        dma4s(rm, LB + L_M + w * 4096, (lane >> 1) * 6144 + (lane & 1) * 16, (b & 3) * (128 * 6144) + (b >> 2) * 128 + w * 32, 32 * 6144);
    }
    u32x4 xr[10];
#pragma unroll
    for (int n = 0; n < 10; ++n) {
        int row = (int)rg * 64 - 16 + n * 8; if (row < 0) row += 16;
        xr[n] = ld16<0>(rx, vy, (unsigned)row * 1024 + w * 128);
    }
    const unsigned vq = r8 * 3072 + (swz0 ^ ((w & 1) * 64));
    auto qkv_issue = [&](int r, unsigned slot) {
        const unsigned ks = kh * 8 + ((r + 2 * rot) & 7);
        dma3(rq, LB + slot + kh * 12288 + hd * 3072, vq, (ks * 32 + hd * 8) * 3072 + cg * 128);
    };
    qkv_issue(0, 96 * K); qkv_issue(1, 72 * K);
    STAMP(1);
    if (prod) {
        VMW(20); TIE2(ep, cv);
        const float a = lo16(cv), c = hi16(cv);
        LD(unsigned, L_SC + w * 256 + lane * 4) = pk2(a * __builtin_amdgcn_rcpf(1.f + __builtin_amdgcn_exp2f(-1.44269504089f * a)), c * __builtin_amdgcn_rcpf(1.f + __builtin_amdgcn_exp2f(-1.44269504089f * c)));
        VMW(16);
        f32x4 acc = {0, 0, 0, 0};
        const unsigned a0 = L_M + w * 4096 + (8 * g + q) * 32 + p * 8;
#pragma unroll
        for (int s = 0; s < 4; ++s) acc = mfma32(TRF(a0 + s * 1024, 128), LD(bf16x8, L_SC + w * 256 + s * 64 + g * 16), acc);
        u32x4 v;
#pragma unroll
        for (int j = 0; j < 4; ++j) v[j] = __builtin_amdgcn_perm(F2U(acc[j]), ep + 0x01010101u, 0x07060500u | j);
        st16s(rw, tk == 0 ? g * 16 : OOB, WS_MODP + (b * 64 + w * 16) * 4, v);
    } else if (w == 4) {
        const float NI = -__builtin_inff();
        f32x4 mcur, mprev; bool vc[4], vp[4];
#pragma unroll
        for (int r = 0; r < 4; ++r) {
            const int d = (int)tk - (int)(4 * g + r); vc[r] = d >= 0 && !(d & 1); vp[r] = d <= -2 && !(d & 1);
            mcur[r] = vc[r] ? 0.f : NI; mprev[r] = vp[r] ? 0.f : NI;
        }
        const u32x4 M = {(vp[0] ? 0xffffu : 0u) | (vp[1] ? 0xffff0000u : 0u), (vp[2] ? 0xffffu : 0u) | (vp[3] ? 0xffff0000u : 0u),
                         (vc[0] ? 0xffffu : 0u) | (vc[1] ? 0xffff0000u : 0u), (vc[2] ? 0xffffu : 0u) | (vc[3] ? 0xffff0000u : 0u)};
        LD(f32x4, L_MK + l16) = mcur; LD(f32x4, L_MK + 1024 + l16) = mprev; LD(u32x4, L_MK + 2048 + l16) = M;
        LD(u32x4, L_MK + 3072 + l16) = (u32x4){0x3f803f80u, 0x3f803f80u, 0x3f803f80u, 0x3f803f80u};
    }
    STAMP(2);
#pragma unroll
    for (int h5 = 0; h5 < 2; ++h5) {
        if (h5 == 0) { qkv_issue(2, 48 * K); VMW(14); } else { qkv_issue(3, 24 * K); VMW(12); }
        float s[5], qq[5];
#pragma unroll
        for (int n = 0; n < 5; ++n) {
            TIE1(xr[h5 * 5 + n]);
            const u32x4 v = xr[h5 * 5 + n];
            s[n] = dot2(v[3], 0x3f803f80u, dot2(v[2], 0x3f803f80u, dot2(v[1], 0x3f803f80u, dot2(v[0], 0x3f803f80u, 0.f))));
            qq[n] = dot2(v[3], v[3], dot2(v[2], v[2], dot2(v[1], v[1], dot2(v[0], v[0], 0.f))));
        }
#define A5(F) F(0) F(1) F(2) F(3) F(4)
#define ST_A(n) "v_add_f32_dpp %" #n ", %" #n ", %" #n " row_half_mirror row_mask:0xf bank_mask:0x5\n\t"
#define ST_C(n) "v_add_f32_dpp %" #n ", %" #n ", %" #n " quad_perm:[1,0,3,2] row_mask:0xf bank_mask:0xf\n\t"
#define ST_D(n) "v_add_f32_dpp %" #n ", %" #n ", %" #n " quad_perm:[2,3,0,1] row_mask:0xf bank_mask:0xf\n\t"
#define ST_B(n, m) "v_add_f32_dpp %" #n ", %" #m ", %" #m " row_half_mirror row_mask:0xf bank_mask:0xa\n\t"
        asm volatile("s_nop 1\n\t" A5(ST_A) ST_B(0, 5) ST_B(1, 6) ST_B(2, 7) ST_B(3, 8) ST_B(4, 9) A5(ST_C) A5(ST_D)
                     : "+v"(s[0]), "+v"(s[1]), "+v"(s[2]), "+v"(s[3]), "+v"(s[4]) : "v"(qq[0]), "v"(qq[1]), "v"(qq[2]), "v"(qq[3]), "v"(qq[4]));
        unsigned sa = L_STAT + r8 * 64 + w * 8 + (c8 >> 2) * 4; PIN(sa);
#pragma unroll
        for (int n = 0; n < 5; ++n) LD(float, sa + (h5 * 5 + n) * 512) = s[n];
    }
    TIE1(ep);
    const unsigned tag = ep + 0x01010101u;
    qkv_issue(4, 0);
    STAMP(3);
    VMW(9); BAR();
    STAMP(4);
    qkv_issue(7, 120 * K);
    bf16x8 Wq[8][3];
    const unsigned trq = kh * 12288 + g * 3072 + q * 128 + (((2 * hd) ^ (fl << 1)) + (p >> 1)) * 16 + (p & 1) * 8;
    unsigned trqh = trq + 72 * K; PIN(trqh);
    auto qkv_read = [&](int r, unsigned base, unsigned slot) {
#pragma unroll
        for (int m = 0; m < 3; ++m) Wq[r][m] = TRF(base + slot + m * 1024, 512);
    };
    if (w == 4 || (w == 5 && lane < 16)) {
        const unsigned row = (w - 4) * 64 + lane;
        const f32x4 a = LD(f32x4, L_STAT + row * 64) + LD(f32x4, L_STAT + row * 64 + 16) + LD(f32x4, L_STAT + row * 64 + 32) + LD(f32x4, L_STAT + row * 64 + 48);
        const float mu = (a[0] + a[2]) * (1.f / 512.f);
        const float var = __builtin_fmaxf((a[1] + a[3]) * (1.f / 512.f) - mu * mu, 0.f);
        const float rs = __builtin_amdgcn_rsqf(var + 1e-5f);
        LD(f32x2, L_RST + row * 8) = (f32x2){rs, -mu * rs};
    }
    qkv_read(0, trqh, 24 * K); qkv_read(1, trqh, 0);
    float t[10][8];
#pragma unroll
    for (int n = 0; n < 10; ++n)
#pragma unroll
        for (int j = 0; j < 4; ++j) { t[n][2 * j] = lo16(xr[n][j]); t[n][2 * j + 1] = hi16(xr[n][j]); PIN(t[n][2 * j]); PIN(t[n][2 * j + 1]); }
    BAR();
    STAMP(5);
    qkv_issue(5, 96 * K); qkv_issue(6, 72 * K);
    {
        unsigned ra = L_RST + r8 * 8; PIN(ra);
#pragma unroll
        for (int n = 0; n < 10; ++n) {
            const f32x2 st = LD(f32x2, ra + n * 64);
#pragma unroll
            for (int j = 0; j < 8; ++j) { t[n][j] = __builtin_fmaf(t[n][j], st.x, st.y); PIN(t[n][j]); }
        }
    }
    const unsigned tile0 = cg * 11 > 75 ? 75 : cg * 11;
    {
        const u32x4 ff = {~0u, ~0u, ~0u, ~0u};
        st16s(rw, l16, WS_O + ((rg * 4 + (w >> 1)) * 16 + cg * 2 + (w & 1)) * 1024, ff);
        st16s(rw, vy, WS_Y + (rg * 64 + w * 8) * 1024 + cg * 128, ff);
        const unsigned ub = WS_U + (rg * 4 + (w >> 1)) * 44032 + tile0 * 512 + (w & 1) * 3072;
        st16s(rw, l16, (cg < 7 || (w & 1)) ? ub : OOB, ff);
        st16s(rw, l16, ub + 1024, ff);
        const unsigned lo32 = lane < 32 ? 0u : OOB;
        st16s(rw, l16 | ((w & 1) ? lo32 : 0u), ub + 2048, ff);
        st16s(rw, vy | lo32, w == 0 ? WS_ST + (rg * 32 + cg) * 128 : OOB, ff);
    }
    STAMP(6);
    VMW(15); BAR();
    STAMP(7);
    qkv_read(2, trq, 48 * K); qkv_read(3, trq, 24 * K); qkv_read(4, trq, 0);
    auto mod_ld = [&](unsigned g0, unsigned g1, u32x4& a, u32x4& c) {
        a = ld16c<0>(rw, l16, WS_MODP + g0 * 1024); c = ld16c<0>(rw, l16, WS_MODP + g1 * 1024);
    };
    auto mod_bad = [&](u32x4 a, u32x4 c) {
        const unsigned ta = __builtin_amdgcn_perm(__builtin_amdgcn_perm(a[3], a[2], 0x0c0c0400u), __builtin_amdgcn_perm(a[1], a[0], 0x0c0c0400u), 0x05040100u);
        const unsigned tc = __builtin_amdgcn_perm(__builtin_amdgcn_perm(c[3], c[2], 0x0c0c0400u), __builtin_amdgcn_perm(c[1], c[0], 0x0c0c0400u), 0x05040100u);
        return anyl(ta != tag || tc != tag);
    };
    auto mod_fin = [&](unsigned g0, unsigned g1, u32x4 a, u32x4 c, float add1) {
#pragma nounroll
        for (int it = 0; it < SPIN && mod_bad(a, c); ++it) { mod_ld(g0, g1, a, c); VMW(0); TIE2(a, c); }
        float s[4];
#pragma unroll
        for (int j = 0; j < 4; ++j) s[j] = sw16add(U2F(a[j] & 0xffffff00u), U2F(c[j] & 0xffffff00u));
        const unsigned r = pk2(sw32add(s[0], s[2]), sw32add(s[1], s[3]));
        const float ad = (g & 1) ? add1 : 0.f;
        return pk2(lo16(r) + ad, hi16(r) + ad);
    };
    u32x4 ma, mc;
    mod_ld(w, 8 + w, ma, mc);
    VMW(2); BAR();
    STAMP(8);
    st4s(rw, lane == 0 ? 0u : OOB, w == 0 ? WS_FLAG + (rg * 8 + cg) * 4 : OOB, tag);
    dma4(ro, LB + 32 * K + w * 4096, vo1k0, vo1k1, (w * 64) * 1024 + cg * 128);
    qkv_read(5, trqh, 24 * K); qkv_read(6, trqh, 0); qkv_read(7, trqh, 48 * K);
    VMW(5); TIE2(ma, mc);
    f32x4 A0, A1, S0, S1;
    {
        const unsigned r = mod_fin(w, 8 + w, ma, mc, 1.f);
        LD(f32x2, L_AS + w * 512 + (g & 1) * 256 + tk * 16 + (g >> 1) * 8) = (f32x2){lo16(r), hi16(r)};
        unsigned aa = L_AS + w * 512 + c8 * 32; PIN(aa);
        S0 = LD(f32x4, aa); S1 = LD(f32x4, aa + 16); A0 = LD(f32x4, aa + 256); A1 = LD(f32x4, aa + 272);
    }
    STAMP(9);
    const unsigned wrot = kh * 4 + ((w - rot) & 3);
    unsigned aw0 = ((2 * wrot + (c8 >> 2)) * 64 + (c8 & 3) * 16 + (r8 ^ ((c8 & 3) | ((c8 >> 2) * 12)))) * 16, aw1 = aw0 ^ 128;
    unsigned ar0 = (kh * 512 + g * 16 + (tk ^ g)) * 16, ar1 = (kh * 512 + 64 + g * 16 + (tk ^ (g | 12))) * 16;
    PIN(aw0); PIN(aw1); PIN(ar0); PIN(ar1);
    constexpr unsigned HS[5] = {0, 16 * K, 32 * K, 48 * K, 32 * K};
    auto norm1 = [&](int n) {
        const float* tt = t[n];
        const u32x4 h = {pk2(__builtin_fmaf(tt[0], A0[0], S0[0]), __builtin_fmaf(tt[1], A0[1], S0[1])), pk2(__builtin_fmaf(tt[2], A0[2], S0[2]), __builtin_fmaf(tt[3], A0[3], S0[3])),
                         pk2(__builtin_fmaf(tt[4], A1[0], S1[0]), __builtin_fmaf(tt[5], A1[1], S1[1])), pk2(__builtin_fmaf(tt[6], A1[2], S1[2]), __builtin_fmaf(tt[7], A1[3], S1[3]))};
        LD(u32x4, HS[n >> 1] + ((n & 1) ? aw1 : aw0)) = h;
    };
    norm1(0); norm1(1);
    BAR();
    STAMP(10);
    bf16x8 hf[8];
    auto h_rd = [&](int s, unsigned base) { hf[s] = LD(bf16x8, base + (s >> 1) * 2048 + ((s & 1) ? ar1 : ar0)); };
    h_rd(0, 0); h_rd(1, 0); h_rd(2, 0); h_rd(3, 0);
    dma4(ro, LB + 64 * K + w * 4096, vo1k0, vo1k1, (w * 64 + 32) * 1024 + cg * 128);
    unsigned v1o[3];
#pragma unroll
    for (int j = 0; j < 3; ++j) { const unsigned ch = (hd + 4 * (j == 2 && hd == 3 ? 1 : j)) * 64 + lane, r = ch / 22; v1o[j] = r * 2752 + (ch - r * 22) * 16; PIN(v1o[j]); }
    auto w1_issue = [&](int r, unsigned slot) {
#pragma unroll
        for (int h2 = 0; h2 < 2; ++h2) {
            const unsigned so = (h2 * 8 + ((r + 2 * rot) & 7)) * (32 * 2752) + tile0 * 32, m0 = LB + slot + h2 * 11264 + hd * 1024;
            dma1(r1, m0, v1o[0], so); dma1(r1, m0 + 4096, v1o[1], so);
            dma1(r1, m0 + (hd < 3 ? 8192 : 4096), v1o[2], so);
        }
    };
    bf16x8 Wo[2][4], W1[8][3], W2[6][4];
    auto row_read = [&](bf16x8* W, unsigned base) {
        W[0] = TRF(base + tre, 512); W[1] = TRF(base + tro, 512); W[2] = TRF(base + (tre ^ 64), 512); W[3] = TRF(base + (tro ^ 64), 512);
    };
    unsigned tr1 = 74 * K + kh * 11264 + (4 * g + q) * 352 + hd * 96 + p * 8; PIN(tr1);
    auto w1_read = [&](int r, unsigned slot) {
#pragma unroll
        for (int m = 0; m < 3; ++m) W1[r][m] = TRF(tr1 + (slot - 74 * K) + m * 32, 5632);
    };

    unsigned xp = L_XCH + hd * 4096 + l16; PIN(xp);
    unsigned mkp = L_MK + l16; PIN(mkp);
    PIN(vos);
    u32x2 kc, vc;
    u32x4 m2a, m2c, m3a, m3c; unsigned flg;
    const unsigned cbw = (g & 1) * 1024 + (tk >> 3) * 64 + (tk & 3) * 16 + ((tk >> 2) & 1) * 8 + (g >> 1) * 4;
    f32x4 aq, ak, av;
    auto attn = [&](int T) {
        const f32x4 pk_ = LD(f32x4, xp + 1024), pv_ = LD(f32x4, xp + 2048);
        if (T == 0) {
            kc = (u32x2){pk2(ak[0] + pk_[0], ak[1] + pk_[1]), pk2(ak[2] + pk_[2], ak[3] + pk_[3])};
            vc = (u32x2){pk2(av[0] + pv_[0], av[1] + pv_[1]), pk2(av[2] + pv_[2], av[3] + pv_[3])};
            return;
        }
        const f32x4 pq_ = LD(f32x4, xp);
        const u32x2 kp = LD(u32x2, xp + 3072), vp = LD(u32x2, xp + 3080);
        const f32x4 mcur = LD(f32x4, mkp); f32x4 mprev = LD(f32x4, mkp + 1024);
        const u32x4 M = LD(u32x4, mkp + 2048), ones = LD(u32x4, mkp + 3072);
        if (T == 1 && UNLIKELY(rg == 0)) { const float NI = -__builtin_inff(); mprev = (f32x4){NI, NI, NI, NI}; PIN(mprev); }
        const unsigned q0 = pk2(aq[0] + pq_[0], aq[1] + pq_[1]), q1 = pk2(aq[2] + pq_[2], aq[3] + pq_[3]);
        const f32x4 sp = mfma16u(kp[0], kp[1], q0, q1, mprev);
        kc = (u32x2){pk2(ak[0] + pk_[0], ak[1] + pk_[1]), pk2(ak[2] + pk_[2], ak[3] + pk_[3])};
        const f32x4 sc = mfma16u(kc[0], kc[1], q0, q1, mcur);
        vc = (u32x2){pk2(av[0] + pv_[0], av[1] + pv_[1]), pk2(av[2] + pv_[2], av[3] + pv_[3])};
        const float t0 = fmx(fmx(fmx(sp[0], sp[1]), sc[0]), sc[1]), t1 = fmx(fmx(fmx(sp[2], sp[3]), sc[2]), sc[3]);
        const float C = 0.25f * 1.44269504089f;
        const float mx = xmax32(xmax16(fmx(t0, t1))) * C;
        const float e0 = __builtin_amdgcn_exp2f(__builtin_fmaf(t0, C, -mx)), e1 = __builtin_amdgcn_exp2f(__builtin_fmaf(t1, C, -mx));
        const unsigned p0 = pk2(e0, e0), p1 = pk2(e1, e1);
        const u32x4 P = {p0 & M[0], p1 & M[1], p0 & M[2], p1 & M[3]};
        const f32x4 sm = mfma32u(ones, P, (f32x4){0, 0, 0, 0});
        const f32x4 o = mfma32u((u32x4){vp[0], vp[1], vc[0], vc[1]}, P, (f32x4){0, 0, 0, 0});
        const float inv = __builtin_amdgcn_rcpf(sm[0]);
        const unsigned oa = pk2(o[0] * inv, o[1] * inv), ob = pk2(o[2] * inv, o[3] * inv);
        auto ra = __builtin_amdgcn_permlane16_swap(oa, oa, false, false);
        auto rb = __builtin_amdgcn_permlane16_swap(ob, ob, false, false);
        st16s(rw, vos, WS_O + ((rg * 4 + T - 1) * 16 + cg * 2) * 1024 + hd * 512, (u32x4){ra[0], rb[0], ra[1], rb[1]});
    };
#pragma unroll
    for (int T = 0; T < 5; ++T) {
        const bool own = kh == (unsigned)(T & 1);
        if (T > 0) { h_rd(0, HS[T]); h_rd(1, HS[T]); h_rd(2, HS[T]); h_rd(3, HS[T]); }
        if (own) {
            if (T == 2 || T == 3) mod_ld(24 + w, 32 + w, m2a, m2c);
            if (T == 3) mod_ld(16 + cg, 40 + cg, m3a, m3c);
            if (T == 4) flg = ld4c(rw, c8 * 4, WS_FLAG + rg * 32);
        } else if (T > 0) attn(T - 1);
        SB(); STAMP(40 + T * 4); SB();
        aq = (f32x4){0, 0, 0, 0}; ak = aq; av = aq;
#pragma unroll
        for (int s = 0; s < 8; ++s) {
            if (T > 0) aq = mfma32(Wq[s][0], hf[s], aq);
            if (s < 4) h_rd(s + 4, HS[T]);
            ak = mfma32(Wq[s][1], hf[s], ak);
            av = mfma32(hf[s], Wq[s][2], av);
            if (T == 0 && (s & 3) == 1) norm1(2 + (s >> 2));
            if (T == 1 && (s & 1) == 0) norm1(4 + (s >> 1));
            if (T == 3 && (s & 3) == 1) norm1(8 + (s >> 2));
            if (T == 3 && s == 6) { w1_read(0, 96 * K); w1_read(1, 118 * K); }
            if (T == 4 && s == 4) w1_read(2, 74 * K);
            if (s & 1) SB();
        }
        STAMP(41 + T * 4);
        if (T == 0) { if (kh == 0) w1_issue(0, 96 * K); else w1_issue(1, 118 * K); STAMP(60); VMW(10); STAMP(42); row_read(Wo[0], 32 * K); }
        if (T == 1) { VMW(6); STAMP(46); row_read(Wo[1], 64 * K); }
        if (!own) {
            if (T > 0) { LD(f32x4, xp) = aq; LD(u32x2, xp + 3072) = kc; LD(u32x2, xp + 3080) = vc; }
            LD(f32x4, xp + 1024) = ak; LD(f32x4, xp + 2048) = av;
            if (T >= 2) VMW(1);
        } else {
            if (T == 2 || T == 3) {
                VMW(0);
                if (T == 2) w1_issue(2, 74 * K); else w1_issue(3, 0);
                if (T == 3) { TIE2(m3a, m3c); if (w == 7) LD(unsigned, L_GT + cbw - (g & 1) * 896) = mod_fin(16 + cg, 40 + cg, m3a, m3c, 0.f); }
                TIE2(m2a, m2c);
                LD(unsigned, L_CB + wrot * 128 + cbw) = mod_fin(24 + w, 32 + w, m2a, m2c, 1.f);
            }
            if (T == 4) {
                VMW(0); TIE1(flg);
#pragma nounroll
                for (int it = 0; it < SPIN && anyl(flg != tag); ++it) { flg = ld4c(rw, c8 * 4, WS_FLAG + rg * 32); VMW(0); TIE1(flg); }
                w1_issue(4, 96 * K); w1_issue(5, 118 * K);
            }
        }
        STAMP(43 + T * 4);
        BAR();
        STAMP(11 + T);
    }

    RELANE();
    const unsigned fb = (lane >> 3) & 1, fg = (lane >> 1) & 3, fe = lane & 1, ftok = hd * 4 + g;
    unsigned fvo = ftok * 1024 + fb * 64 + fe * 32 + fg * 8; PIN(fvo);
    unsigned pw0 = w * 4096 + tk * 256 + (g ^ (tk & 7)) * 16, pw1 = pw0 ^ 64;
    unsigned pr = (kh ^ 1) * 32768 + ftok * 256 + ((fb * 8 + fe * 4 + fg) ^ (ftok & 7)) * 16;
    PIN(pw0); PIN(pw1); PIN(pr);
    const unsigned ks0 = w * 5 + (w < 3 ? w : 3);
    auto w2_issue = [&](int s, unsigned slot) { dma4(r2, LB + slot + w * 4096, vo1k0, vo1k1, (ks0 + s) * 32768 + cg * 128); };
    u32x4 of[4][2]; u32x2 xres[2], x1p[2];
    auto o_ld = [&](int tt) { const unsigned so = WS_O + ((rg * 4 + tt) * 16 + 2 * w) * 1024; of[tt][0] = ld16c<0>(rw, l16, so); of[tt][1] = ld16c<1024>(rw, l16, so); };
    f32x4 sacc[3] = {{0, 0, 0, 0}, {0, 0, 0, 0}, {0, 0, 0, 0}};
    auto s1_part = [&](int h) {
        unsigned ca = L_CB + (tk & 1) * 1024 + kh * 512 + g * 16; PIN(ca);
        bf16x8 yf[4];
#pragma unroll
        for (int s = 0; s < 4; ++s) yf[s] = LD(bf16x8, ca + (h * 4 + s) * 64);
#pragma unroll
        for (int s = 0; s < 4; ++s)
#pragma unroll
            for (int m = 0; m < 3; ++m) sacc[m] = mfma32(W1[h * 4 + s][m], yf[s], sacc[m]);
        if (h == 1 && tk < 2) {
#pragma unroll
            for (int m = 0; m < 3; ++m) LD(f32x4, L_S1P + kh * 1536 + tk * 768 + (hd * 48 + m * 16 + g * 4) * 4) = sacc[m];
        }
    };
    f32x4 ga, A2;
    auto fin_wo = [&](int tt) {
        f32x4 pp[8];
#pragma unroll
        for (int ww = 0; ww < 8; ++ww) pp[ww] = LD(f32x4, pr + ww * 4096);
        if (tt < 2) {
            const u32x2 gv = LD(u32x2, L_GT + fb * 64 + fg * 16 + fe * 8);
            const u32x2 av2 = LD(u32x2, L_CB + 1024 + ((cg & 4) + ((cg - rot) & 3)) * 128 + fb * 64 + fg * 16 + fe * 8);
            ga = (f32x4){lo16(gv[0]), hi16(gv[0]), lo16(gv[1]), hi16(gv[1])};
            A2 = (f32x4){lo16(av2[0]), hi16(av2[0]), lo16(av2[1]), hi16(av2[1])};
        }
        const f32x4 sum = ((pp[0] + pp[1]) + (pp[2] + pp[3])) + ((pp[4] + pp[5]) + (pp[6] + pp[7]));
        const u32x2 xr2 = xres[tt >> 1];
        const f32x4 xv = {lo16(xr2[0]), hi16(xr2[0]), lo16(xr2[1]), hi16(xr2[1])};
        const f32x4 v = ga * sum + xv;
        const u32x2 x1 = {pk2(v[0], v[1]), pk2(v[2], v[3])};
        x1p[tt >> 1] = x1;
        const f32x4 xx = (f32x4){lo16(x1[0]), hi16(x1[0]), lo16(x1[1]), hi16(x1[1])} * A2;
        const unsigned y0 = pk2(xx[0], xx[1]), y1 = pk2(xx[2], xx[3]);
        const u32x4 yy = {y0, y1, dppu<0xF5>(y0), dppu<0xF5>(y1)};
        st16s(rw, fe ? OOB : ftok * 1024 + tk * 8, WS_Y + (rg * 4 + tt) * 16384 + cg * 128, yy);
        float s = dot2(x1[1], 0x3f803f80u, dot2(x1[0], 0x3f803f80u, 0.f));
        const float qq = dot2(x1[1], x1[1], dot2(x1[0], x1[0], 0.f));
        asm volatile("s_nop 1\n\tv_add_f32_dpp %0, %0, %0 row_ror:8 row_mask:0xf bank_mask:0x3\n\tv_add_f32_dpp %0, %1, %1 row_ror:8 row_mask:0xf bank_mask:0xc\n\ts_nop 1" : "+v"(s) : "v"(qq));
        s += dpp<0x141>(s); s += dpp<0xB1>(s); s += dpp<0x4E>(s);
        st4s(rw, c8 ? OOB : ftok * 8 + fb * 4, WS_ST + ((rg * 4 + tt) * 8 + cg) * 128, F2U(s));
    };
    auto wo_step = [&](int tt) {
        TIE2(of[tt][0], of[tt][1]);
#pragma nounroll
        for (int it = 0; it < SPIN && anyl(umax4(of[tt][1], umax4(of[tt][0], 0)) == ~0u); ++it) { o_ld(tt); VMW(0); TIE2(of[tt][0], of[tt][1]); }
        STAMP(16 + tt * 2);
        f32x4 acc[4];
#pragma unroll
        for (int c = 0; c < 4; ++c) acc[c] = mfma32(Wo[0][c], __builtin_bit_cast(bf16x8, of[tt][0]), (f32x4){0, 0, 0, 0});
#pragma unroll
        for (int c = 0; c < 4; ++c) acc[c] = mfma32(Wo[1][c], __builtin_bit_cast(bf16x8, of[tt][1]), acc[c]);
#pragma unroll
        for (int c = 0; c < 4; ++c) LD(f32x4, ((tt & 1) ^ 1) * 32768 + (c >> 1) * 128 + ((c & 1) ? pw1 : pw0)) = acc[c];
    };
    o_ld(0); o_ld(1);
    xres[0] = ld8(rx, fvo, (rg * 64 + kh * 16) * 1024 + cg * 128); xres[1] = ld8(rx, fvo, (rg * 64 + 32 + kh * 16) * 1024 + cg * 128);
    w1_read(3, 0);
    if (kh == 0) { attn(4); VMW(1); } else { w1_issue(6, 74 * K); s1_part(0); VMW(6); }
    TIE2(xres[0], xres[1]);
    wo_step(0);
    BAR();
    STAMP(17);
    o_ld(2);
    if (kh == 0) fin_wo(0);
    w1_read(4, 96 * K); w1_read(5, 118 * K);
    wo_step(1);
    if (kh == 1) VMW(2);
    BAR();
    STAMP(19);
    o_ld(3);
    w2_issue(0, L_W2A);
    if (kh == 1) { fin_wo(1); VMW(8); } else { w1_issue(7, 96 * K); s1_part(0); VMW(12); }
    w1_read(6, 74 * K);
    wo_step(2);
    if (kh == 0) VMW(0);
    BAR();
    STAMP(21);
    w1_read(7, 96 * K);
    if (kh == 0) fin_wo(2); else s1_part(1);
    VMW(2);
    row_read(W2[0], L_W2A);
    wo_step(3);
    asm volatile("s_waitcnt lgkmcnt(0)" ::: "memory");
    w2_issue(1, L_W2A);
    BAR();
    STAMP(23);

    RELANE();
    aw0 += L_ACT; aw1 += L_ACT; ar0 += L_ACT; ar1 += L_ACT; PIN(aw0); PIN(aw1); PIN(ar0); PIN(ar1);
    u32x4 yv[2], sv;
    auto y_ld = [&](int tt) {
        const unsigned so = WS_Y + (rg * 4 + tt) * 16384 + w * 128;
        yv[0] = ld16c<0>(rw, vy, so); yv[1] = ld16c<0>(rw, vy, so + 8192);
        sv = ld16c<0>(rw, l16, (w == (unsigned)(((tt + 1) & 1) * 4)) ? WS_ST + (rg * 4 + tt) * 1024 : OOB);
    };
    auto y_put = [&](int tt) {
        TIE2(yv[0], yv[1]); TIE1(sv);
#pragma nounroll
        for (int it = 0; it < SPIN && anyl(umax4(sv, umax4(yv[1], umax4(yv[0], 0))) == ~0u); ++it) { y_ld(tt); VMW(0); TIE2(yv[0], yv[1]); TIE1(sv); }
        LD(u32x4, (tt & 1) * 16384 + aw0) = yv[0]; LD(u32x4, (tt & 1) * 16384 + aw1) = yv[1];
        if (w == (unsigned)(((tt + 1) & 1) * 4)) {
            float s = sw16add(U2F(sv[0]), U2F(sv[2])), qq = sw16add(U2F(sv[1]), U2F(sv[3]));
            s += dpp<0x128>(s); qq += dpp<0x128>(qq);
            s = sw32add(s, s); qq = sw32add(qq, qq);
            const float mu = s * (1.f / 512.f);
            const float var = __builtin_fmaxf(qq * (1.f / 512.f) - mu * mu, 0.f);
            LD(f32x2, L_SST + tt * 128 + (c8 * 2 + (g & 1)) * 8) = (f32x2){-mu, -1.44269504089f * __builtin_amdgcn_rsqf(var + 1e-5f)};
        }
    };
    y_ld(0);
    if (kh == 1) { fin_wo(3); VMW(2); } else { s1_part(1); VMW(0); }
    row_read(W2[1], L_W2A);
    y_put(0);
    asm volatile("s_waitcnt lgkmcnt(0)" ::: "memory");
    w2_issue(2, L_W2A);
    BAR();
    STAMP(24);
    unsigned xp1 = L_XC1 + hd * 3072 + l16; PIN(xp1);
    unsigned s1a = L_S1 + hd * 192 + g * 16; PIN(s1a);
    unsigned vus0 = (g >> 1) * 256 + tk * 16 + (g & 1) * 512; PIN(vus0);
    u32x4 uf[2][6];
    auto u_ld = [&](int tt) {
        const unsigned so = WS_U + ((rg * 4 + tt) * 43 + ks0) * 1024;
        u32x4* u = uf[tt & 1];
        u[0] = ld16c<0>(rw, l16, so); u[1] = ld16c<1024>(rw, l16, so); u[2] = ld16c<2048>(rw, l16, so); u[3] = ld16c<3072>(rw, l16, so);
        u[4] = ld16c<0>(rw, l16, so + 4096); u[5] = ld16c<0>(rw, l16, w < 3 ? so + 5120 : OOB);
    };
    f32x4 acc[3];
    auto silu = [&](int tt) {
        const f32x2 st = LD(f32x2, L_SST + tt * 128 + tk * 8);
        f32x4 pa[3], t1[3], s1[3];
#pragma unroll
        for (int m = 0; m < 3; ++m) { pa[m] = LD(f32x4, xp1 + m * 1024); s1[m] = LD(f32x4, s1a + 768 + m * 64); t1[m] = LD(f32x4, s1a + m * 64); }
        unsigned ua[3], ub[3];
#pragma unroll
        for (int m = 0; m < 3; ++m) {
            float v[4];
#pragma unroll
            for (int r = 0; r < 4; ++r) {
                const float z = __builtin_fmaf(st.y, __builtin_fmaf(st.x, s1[m][r], acc[m][r] + pa[m][r]), t1[m][r]);
                v[r] = z * __builtin_amdgcn_rcpf(1.f + __builtin_amdgcn_exp2f(z));
            }
            ua[m] = pk2(v[0], v[1]); ub[m] = pk2(v[2], v[3]);
        }
        const unsigned so = WS_U + (rg * 4 + tt) * 44032 + (tile0 + hd * 3) * 512;
        {
            auto ra = __builtin_amdgcn_permlane16_swap(ua[0], ua[1], false, false);
            auto rb = __builtin_amdgcn_permlane16_swap(ub[0], ub[1], false, false);
            st16s(rw, vus0, (cg < 7 || hd > 0) ? so : OOB, (u32x4){ra[0], rb[0], ra[1], rb[1]});
        }
        {
            auto ra = __builtin_amdgcn_permlane16_swap(ua[2], ua[2], false, false);
            auto rb = __builtin_amdgcn_permlane16_swap(ub[2], ub[2], false, false);
            st16s(rw, vos, hd < 3 ? so + 1024 : OOB, (u32x4){ra[0], rb[0], ra[1], rb[1]});
        }
    };
#pragma unroll
    for (int tt = 0; tt < 4; ++tt) {
        const bool own = kh == (unsigned)(tt & 1);
        const unsigned ab = (tt & 1) * 16384;
        h_rd(0, ab); h_rd(1, ab); h_rd(2, ab); h_rd(3, ab);
        if (tt < 3) y_ld(tt + 1);
        if (tt == 0) {
            if (w == 0 || (w == 1 && lane < 32)) {
                const unsigned o = w * 1024 + l16;
                f32x4 v = LD(f32x4, L_S1P + o) + LD(f32x4, L_S1P + 1536 + o);
                if (o < 768) v *= -1.44269504089f;
                LD(f32x4, L_S1 + o) = v;
            }
        } else if (!own) silu(tt - 1);
        SB();
        acc[0] = (f32x4){0, 0, 0, 0}; acc[1] = acc[0]; acc[2] = acc[0];
#pragma unroll
        for (int s = 0; s < 8; ++s) {
            acc[0] = mfma32(W1[s][0], hf[s], acc[0]);
            if (s < 4) h_rd(s + 4, ab);
            acc[1] = mfma32(W1[s][1], hf[s], acc[1]);
            acc[2] = mfma32(W1[s][2], hf[s], acc[2]);
            if (tt == 0 && s == 2) w2_issue(3, 0);
            if (tt == 0 && s == 5) w2_issue(4, 32 * K);
            if (tt == 1 && s == 3) w2_issue(5, L_W2A);
            SB();
        }
        if (!own) {
#pragma unroll
            for (int m = 0; m < 3; ++m) LD(f32x4, xp1 + m * 1024) = acc[m];
        }
        if (tt == 0) { VMW(8); row_read(W2[2], L_W2A); }
        if (tt == 1) { if (own) VMW(4); else VMW(6); }
        if (tt == 2) { if (own) VMW(0); else VMW(2); }
        if (tt < 3) y_put(tt + 1); else u_ld(0);
        BAR();
        STAMP(25 + tt);
    }

    RELANE();
    u_ld(1);
    if (kh == 1) silu(3);
    STAMP(29);
    row_read(W2[3], 0); row_read(W2[4], 32 * K); row_read(W2[5], L_W2A);
    f32x4 gb;
    {
        const u32x2 gv = LD(u32x2, L_GT + 128 + fb * 64 + fg * 16 + fe * 8);
        gb = (f32x4){lo16(gv[0]), hi16(gv[0]), lo16(gv[1]), hi16(gv[1])} * (-1.f / 1.44269504089f);
    }
    pw0 += L_ACT; pw1 += L_ACT; pr = (pr ^ 32768) + L_ACT; PIN(pw0); PIN(pw1); PIN(pr);
    auto fin_out = [&](int tt) {
        f32x4 pp[8];
#pragma unroll
        for (int ww = 0; ww < 8; ++ww) pp[ww] = LD(f32x4, pr + ww * 4096);
        const f32x4 sum = ((pp[0] + pp[1]) + (pp[2] + pp[3])) + ((pp[4] + pp[5]) + (pp[6] + pp[7]));
        const u32x2 xp2 = x1p[tt >> 1];
        const f32x4 v = gb * sum + (f32x4){lo16(xp2[0]), hi16(xp2[0]), lo16(xp2[1]), hi16(xp2[1])};
        __builtin_amdgcn_raw_buffer_store_b64((u32x2){pk2(v[0], v[1]), pk2(v[2], v[3])}, rout, (int)fvo, (int)((rg * 4 + tt) * 16384 + cg * 128), 0);
    };
#pragma unroll
    for (int tt = 0; tt < 4; ++tt) {
        u32x4* u = uf[tt & 1];
        if (tt < 3) VMW(6); else VMW(0);
        TIE2(u[0], u[1]); TIE2(u[2], u[3]); TIE2(u[4], u[5]);
#pragma nounroll
        for (int it = 0; it < SPIN && anyl(umax4(u[5], umax4(u[4], umax4(u[3], umax4(u[2], umax4(u[1], umax4(u[0], 0)))))) == ~0u); ++it) {
            u_ld(tt); VMW(0); TIE2(u[0], u[1]); TIE2(u[2], u[3]); TIE2(u[4], u[5]);
        }
        STAMP(30 + tt * 2);
        f32x4 a2[4] = {{0, 0, 0, 0}, {0, 0, 0, 0}, {0, 0, 0, 0}, {0, 0, 0, 0}};
        if (w < 3) {
#pragma unroll
            for (int c = 0; c < 4; ++c) a2[c] = mfma32(W2[5][c], __builtin_bit_cast(bf16x8, u[5]), a2[c]);
        }
#pragma unroll
        for (int s = 0; s < 5; ++s)
#pragma unroll
            for (int c = 0; c < 4; ++c) a2[c] = mfma32(W2[s][c], __builtin_bit_cast(bf16x8, u[s]), a2[c]);
        SB();
        if (tt < 2) u_ld(tt + 2);
#pragma unroll
        for (int c = 0; c < 4; ++c) LD(f32x4, (tt & 1) * 32768 + (c >> 1) * 128 + ((c & 1) ? pw1 : pw0)) = a2[c];
        BAR();
        STAMP(31 + tt * 2);
        if (kh == (unsigned)(tt & 1)) fin_out(tt);
    }
    if (b == 0 && w == 0) {
#pragma nounroll
        for (int it = 0; it < SPIN; ++it) {
            u32x4 f = ld16c<0>(rw, l16, WS_FLAG); VMW(0); TIE1(f);
            if (!anyl(f[0] != tag || f[1] != tag || f[2] != tag || f[3] != tag)) break;
        }
        st4s(rw, lane == 0 ? 0u : OOB, WS_EPOCH, tag);
    }
    STAMP(38); STAMPC(63);
}

#if defined(__HIP_DEVICE_COMPILE__) && !defined(PK)
#pragma clang attribute pop
#endif
extern "C" void kernel_launch(void* const* d_in, const int* in_sizes, int n_in, void* d_out, int out_size, void* d_ws, size_t ws_size, hipStream_t stream) {
    fused_block<<<256, 512, 0, stream>>>((const char*)d_in[0], (const char*)d_in[1], (const char*)d_in[2], (const char*)d_in[4], (const char*)d_in[5],
                                         (const char*)d_in[6], (const char*)d_in[7], (char*)d_ws, (char*)d_out);
}
```

```cpp
#include <hip/hip_runtime.h>
#include <stdint.h>

#if defined(__HIP_DEVICE_COMPILE__) && !defined(PK)
#pragma clang attribute push(__attribute__((target("no-packed-fp32-ops"))), apply_to = function)
#endif
#define LAS __attribute__((address_space(3)))
typedef __bf16 bf16x8 __attribute__((ext_vector_type(8)));
typedef __bf16 bf16x4 __attribute__((ext_vector_type(4)));
typedef __bf16 bf16x2 __attribute__((ext_vector_type(2)));
typedef short s16x4 __attribute__((ext_vector_type(4)));
typedef float f32x4 __attribute__((ext_vector_type(4)));
typedef float f32x2 __attribute__((ext_vector_type(2)));
typedef unsigned u32x4 __attribute__((ext_vector_type(4)));
typedef unsigned u32x2 __attribute__((ext_vector_type(2)));
typedef __amdgpu_buffer_rsrc_t rsrc_t;
#define DI __device__ __forceinline__
#define U2F(x) __builtin_bit_cast(float, (unsigned)(x))
#define F2U(x) __builtin_bit_cast(unsigned, (float)(x))
#define EXP 0
#define MAP 1
#define ROT 1

constexpr unsigned WS_EPOCH = 0, WS_FLAG = 4096, WS_MODP = 8192, WS_ST = 65536, WS_O = 1u << 20, WS_Y = 3u << 20, WS_U = 5u << 20, WS_BYTES = 11u << 20;
constexpr int SPIN = 1 << 15;
constexpr unsigned K = 1024;
constexpr unsigned L_M = 0, L_STAT = 144 * K, L_RST = 149 * K, L_SC = 150 * K, L_AS = 152 * K, L_MK = 156 * K;
constexpr unsigned L_H = 121 * K, L_X = 97 * K;
constexpr unsigned L_CB = 153 * K, L_GT = 155 * K, L_SST = 155 * K + 512, L_S1 = 156 * K;
constexpr unsigned L_PW = 121 * K, PWD = 16 * K, L_ACT = 97 * K, L_XC1 = 129 * K;
constexpr unsigned Z0 = 0, Z1 = 11 * K, Z2 = 22 * K, Z3 = 33 * K, Z4 = 44 * K, Z5 = 64 * K, Z6 = 75 * K, Z7 = 86 * K, Z8 = 97 * K, Z9 = 108 * K;

DI unsigned pk2(float a, float b) { bf16x2 v = {(__bf16)a, (__bf16)b}; return __builtin_bit_cast(unsigned, v); }
DI float lo16(unsigned u) { return U2F(u << 16); }
DI float hi16(unsigned u) { return U2F(u & 0xffff0000u); }
template <int CTRL> DI float dpp(float v) { return U2F(__builtin_amdgcn_update_dpp(0, (int)F2U(v), CTRL, 0xf, 0xf, true)); }
template <int CTRL> DI unsigned dppu(unsigned v) { return (unsigned)__builtin_amdgcn_update_dpp(0, (int)v, CTRL, 0xf, 0xf, true); }
DI float fmx(float a, float b) { return __builtin_elementwise_maximum(a, b); }
DI float dot2(unsigned a, unsigned b, float c) { return __builtin_amdgcn_fdot2_f32_bf16(__builtin_bit_cast(bf16x2, a), __builtin_bit_cast(bf16x2, b), c, false); }
DI float sw16add(float a, float b) { auto r = __builtin_amdgcn_permlane16_swap(F2U(a), F2U(b), false, false); return U2F(r[0]) + U2F(r[1]); }
DI float sw32add(float a, float b) { auto r = __builtin_amdgcn_permlane32_swap(F2U(a), F2U(b), false, false); return U2F(r[0]) + U2F(r[1]); }
DI float xmax16(float v) { auto r = __builtin_amdgcn_permlane16_swap(F2U(v), F2U(v), false, false); return fmx(U2F(r[0]), U2F(r[1])); }
DI float xmax32(float v) { auto r = __builtin_amdgcn_permlane32_swap(F2U(v), F2U(v), false, false); return fmx(U2F(r[0]), U2F(r[1])); }
DI f32x4 mfma32(bf16x8 a, bf16x8 b, f32x4 c) { return __builtin_amdgcn_mfma_f32_16x16x32_bf16(a, b, c, 0, 0, 0); }
DI f32x4 mfma32u(u32x4 a, u32x4 b, f32x4 c) { return mfma32(__builtin_bit_cast(bf16x8, a), __builtin_bit_cast(bf16x8, b), c); }
DI f32x4 mfma16u(unsigned a0, unsigned a1, unsigned b0, unsigned b1, f32x4 c) {
    return __builtin_amdgcn_mfma_f32_16x16x16bf16_1k(__builtin_bit_cast(s16x4, (u32x2){a0, a1}), __builtin_bit_cast(s16x4, (u32x2){b0, b1}), c, 0, 0, 0);
}
DI rsrc_t mk(const void* p, unsigned n) { return __builtin_amdgcn_make_buffer_rsrc((void*)p, (short)0, (int)n, 0x00020000); }
DI unsigned nosent(unsigned v) { return v < 0xfffffffeu ? v : 0xfffffffeu; }
DI unsigned umax3(unsigned a, unsigned b, unsigned c) { unsigned m = a > b ? a : b; return m > c ? m : c; }
DI unsigned umax4(u32x4 v, unsigned m) { return umax3(umax3(v[0], v[1], m), v[2], v[3]); }

#define BAR() asm volatile("s_waitcnt lgkmcnt(0)\n\ts_barrier" ::: "memory")
#define VMW(n) asm volatile("s_waitcnt vmcnt(%0)" ::"n"(n) : "memory")
#define TIE1(a) asm volatile("; TIE %0" : "+v"(a))
#define TIE2(a, b) asm volatile("; TIE %0 %1" : "+v"(a), "+v"(b))
DI void dma1(rsrc_t r, unsigned m0, unsigned vo, unsigned so) {
    asm volatile("s_mov_b32 m0, %0\n\ts_nop 0\n\tbuffer_load_dwordx4 %1, %2, %3 offen lds" ::"s"(m0), "v"(vo), "s"(r), "s"(so) : "m0", "memory");
}
template <int OFF> DI void dma1o(rsrc_t r, unsigned m0, unsigned vo, unsigned so) {
    asm volatile("s_mov_b32 m0, %0\n\ts_nop 0\n\tbuffer_load_dwordx4 %1, %2, %3 offen offset:%4 lds" ::"s"(m0), "v"(vo), "s"(r), "s"(so), "n"(OFF) : "m0", "memory");
}
DI void dma3(rsrc_t r, unsigned m0, unsigned vo, unsigned so) {
    asm volatile("s_mov_b32 m0, %0\n\ts_nop 0\n\tbuffer_load_dwordx4 %1, %2, %3 offen lds\n\tbuffer_load_dwordx4 %1, %2, %3 offen offset:1024 lds\n\tbuffer_load_dwordx4 %1, %2, %3 offen offset:2048 lds"
                 ::"s"(m0), "v"(vo), "s"(r), "s"(so) : "m0", "memory");
}
DI void dma3v(rsrc_t r, unsigned m0, unsigned v0, unsigned v1, unsigned v2, unsigned so) {
    asm volatile("s_mov_b32 m0, %0\n\ts_nop 0\n\tbuffer_load_dwordx4 %1, %4, %5 offen lds\n\tbuffer_load_dwordx4 %2, %4, %5 offen offset:1024 lds\n\tbuffer_load_dwordx4 %3, %4, %5 offen offset:2048 lds"
                 ::"s"(m0), "v"(v0), "v"(v1), "v"(v2), "s"(r), "s"(so) : "m0", "memory");
}
DI void dma4(rsrc_t r, unsigned m0, unsigned vo0, unsigned vo1, unsigned so) {
    asm volatile("s_mov_b32 m0, %0\n\ts_nop 0\n\tbuffer_load_dwordx4 %1, %3, %4 offen lds\n\tbuffer_load_dwordx4 %2, %3, %5 offen offset:1024 lds\n\t"
                 "buffer_load_dwordx4 %1, %3, %6 offen offset:2048 lds\n\tbuffer_load_dwordx4 %2, %3, %7 offen offset:3072 lds"
                 ::"s"(m0), "v"(vo0), "v"(vo1), "s"(r), "s"(so), "s"(so + 7168u), "s"(so + 14336u), "s"(so + 21504u) : "m0", "memory");
}
template <int OFF> DI u32x4 ld16(rsrc_t r, unsigned vo, unsigned so) {
    u32x4 v; asm volatile("buffer_load_dwordx4 %0, %1, %2, %3 offen offset:%4" : "=v"(v) : "v"(vo), "s"(r), "s"(so), "n"(OFF) : "memory"); return v;
}
template <int OFF> DI u32x4 ld16c(rsrc_t r, unsigned vo, unsigned so) {
    u32x4 v; asm volatile("buffer_load_dwordx4 %0, %1, %2, %3 offen offset:%4 sc1" : "=v"(v) : "v"(vo), "s"(r), "s"(so), "n"(OFF) : "memory"); return v;
}
DI u32x2 ld8(rsrc_t r, unsigned vo, unsigned so) {
    u32x2 v; asm volatile("buffer_load_dwordx2 %0, %1, %2, %3 offen" : "=v"(v) : "v"(vo), "s"(r), "s"(so) : "memory"); return v;
}
DI unsigned ld4(rsrc_t r, unsigned vo, unsigned so) {
    unsigned v; asm volatile("buffer_load_dword %0, %1, %2, %3 offen" : "=v"(v) : "v"(vo), "s"(r), "s"(so) : "memory"); return v;
}
DI unsigned ld4c(rsrc_t r, unsigned vo, unsigned so) {
    unsigned v; asm volatile("buffer_load_dword %0, %1, %2, %3 offen sc1" : "=v"(v) : "v"(vo), "s"(r), "s"(so) : "memory"); return v;
}
DI void st16c(rsrc_t r, unsigned vo, u32x4 v) { __builtin_amdgcn_raw_buffer_store_b128(v, r, (int)vo, 0, 16); }
DI void st4c(rsrc_t r, unsigned vo, unsigned v) { __builtin_amdgcn_raw_buffer_store_b32(v, r, (int)vo, 0, 16); }
DI void st16s(rsrc_t r, unsigned vo, unsigned so, u32x4 v) { __builtin_amdgcn_raw_buffer_store_b128(v, r, (int)vo, (int)so, 16); asm volatile("s_nop 1" ::"v"(v) : "memory"); }
DI void st16n(rsrc_t r, unsigned vo, unsigned so, u32x4 v) { __builtin_amdgcn_raw_buffer_store_b128(v, r, (int)vo, (int)so, 16); }
DI void st4s(rsrc_t r, unsigned vo, unsigned so, unsigned v) { __builtin_amdgcn_raw_buffer_store_b32(v, r, (int)vo, (int)so, 16); }
#define SB() __builtin_amdgcn_sched_barrier(0)
#define PIN(a) asm volatile("" : "+v"(a))
#define UNLIKELY(c) __builtin_expect(!!(c), 0)
constexpr unsigned OOB = 0x40000000u;
DI unsigned newlane() { unsigned l; asm volatile("v_mbcnt_lo_u32_b32 %0, -1, 0\n\tv_mbcnt_hi_u32_b32 %0, -1, %0" : "=v"(l)); return l; }
DI bool anyl(bool c) { return __builtin_amdgcn_ballot_w64(c) != 0; }

#define STAMP(i)
#define STAMPC(i)

#define XS(i)
template <int N> struct IC { static constexpr int v = N; };
template <int I, int N, class F> DI void sfor_(F& f) { if constexpr (I < N) { f(IC<I>{}); sfor_<I + 1, N>(f); } }
template <int I, int N, class F> DI void sfor(F f) { sfor_<I, N>(f); }
#define AI __attribute__((always_inline))
#define IF(c) if constexpr (c)
#define CI(n, c) constexpr int n = decltype(c)::v
DI void dma4s(rsrc_t r, unsigned m0, unsigned vo, unsigned so, unsigned stride) {
    asm volatile("s_mov_b32 m0, %0\n\ts_nop 0\n\tbuffer_load_dwordx4 %1, %2, %3 offen lds\n\tbuffer_load_dwordx4 %1, %2, %4 offen offset:1024 lds\n\t"
                 "buffer_load_dwordx4 %1, %2, %5 offen offset:2048 lds\n\tbuffer_load_dwordx4 %1, %2, %6 offen offset:3072 lds"
                 ::"s"(m0), "v"(vo), "s"(r), "s"(so), "s"(so + stride - 1024u), "s"(so + 2 * (stride - 1024u)), "s"(so + 3 * (stride - 1024u)) : "m0", "memory");
}

__global__ __launch_bounds__(512) void fused_block(const char* x, const char* wqkv, const char* wo, const char* w1, const char* w2,
                                                   const char* cvec, const char* wmod, char* ws, char* out) {
    __shared__ __attribute__((aligned(16))) char lds[163840];
    LAS char* L = (LAS char*)lds;
    const unsigned LB = (unsigned)(uintptr_t)L;
#define LD(T, off) (*(LAS T*)(L + (off)))
#define TRF(off, d) __builtin_shufflevector(__builtin_amdgcn_ds_read_tr16_b64_v4bf16((LAS bf16x4*)(L + (off))), __builtin_amdgcn_ds_read_tr16_b64_v4bf16((LAS bf16x4*)(L + (off) + (d))), 0, 1, 2, 3, 4, 5, 6, 7)
    unsigned lane = __builtin_amdgcn_workitem_id_x() & 63; const unsigned w = __builtin_amdgcn_readfirstlane(__builtin_amdgcn_workitem_id_x() >> 6);
    const unsigned b = __builtin_amdgcn_workgroup_id_x();
    STAMP(0); STAMPC(62);
    const unsigned rg = ((b & 7) >> 1) * 8 + (b >> 5), cg = (b & 1) * 4 + ((b >> 3) & 3);
    const unsigned rot = rg & 3;
    unsigned r8, c8, tk, g, q, p, l16, vy, swz0, fl, vo1k0, vo1k1, tre, tro, vos;
    auto derive = [&]() AI {
        r8 = lane >> 3; c8 = lane & 7; tk = lane & 15; g = lane >> 4; q = (lane & 15) >> 2; p = lane & 3;
        l16 = lane * 16; vy = r8 * 1024 + c8 * 16; swz0 = (c8 ^ (r8 & 2)) * 16; fl = ((q >> 1) & 1) | ((g & 1) << 1);
        vo1k0 = r8 * 1024 + swz0; vo1k1 = vo1k0 ^ 64;
        const unsigned trb = (8 * g + q) * 128 + (p >> 1) * 16 + (p & 1) * 8 + w * 4096;
        tre = trb + (fl << 1) * 16; tro = trb + (2 ^ (fl << 1)) * 16;
        vos = (g & 1) ? OOB : (g >> 1) * 256 + tk * 16;
    };
    derive();
#define RELANE() do { lane = newlane(); derive(); } while (0)
    const unsigned hd = w & 3, kh = w >> 2;
    const rsrc_t rx = mk(x, 2048 * 1024), rq = mk(wqkv, 512 * 3072), ro = mk(wo, 512 * 1024), r1 = mk(w1, 512 * 2752), r2 = mk(w2, 1376 * 1024);
    const rsrc_t rc = mk(cvec, 1024), rm = mk(wmod, 512 * 6144), rw = mk(ws, WS_BYTES), rout = mk(out, 2048 * 1024);
    const bool prod = b < 192 && w < 4;

    unsigned ep = ld4c(rw, 0, WS_EPOCH);
    unsigned cv;
    if (prod) {
        cv = ld4(rc, lane * 4, (b & 3) * 256);
        dma4s(rm, LB + L_M + w * 4096, (lane >> 1) * 6144 + (lane & 1) * 16, (b & 3) * (128 * 6144) + (b >> 2) * 128 + w * 32, 32 * 6144);
    }
    u32x4 xr[10];
#pragma unroll
    for (int n = 0; n < 10; ++n) {
        int row = (int)rg * 64 - 16 + n * 8; if (row < 0) row += 16;
        xr[n] = ld16<0>(rx, vy, (unsigned)row * 1024 + w * 128);
    }
    const unsigned vq = r8 * 3072 + (swz0 ^ ((w & 1) * 64));
    auto qkv_issue = [&](int r, unsigned slot) AI {
        const unsigned ks = kh * 8 + ((r + 2 * rot) & 7);
        dma3(rq, LB + slot + kh * 12288 + hd * 3072, vq, (ks * 32 + hd * 8) * 3072 + cg * 128);
    };
    STAMP(1);
    if (prod) {
        VMW(14); TIE2(ep, cv);
        const float a = lo16(cv), c = hi16(cv);
        LD(unsigned, L_SC + w * 256 + lane * 4) = pk2(a * __builtin_amdgcn_rcpf(1.f + __builtin_amdgcn_exp2f(-1.44269504089f * a)), c * __builtin_amdgcn_rcpf(1.f + __builtin_amdgcn_exp2f(-1.44269504089f * c)));
        VMW(10);
        f32x4 acc = {0, 0, 0, 0};
        const unsigned a0 = L_M + w * 4096 + (8 * g + q) * 32 + p * 8;
#pragma unroll
        for (int s = 0; s < 4; ++s) acc = mfma32(TRF(a0 + s * 1024, 128), LD(bf16x8, L_SC + w * 256 + s * 64 + g * 16), acc);
        u32x4 v;
#pragma unroll
        for (int j = 0; j < 4; ++j) v[j] = __builtin_amdgcn_perm(F2U(acc[j]), ep + 0x01010101u, 0x07060500u | j);
        st16s(rw, tk == 0 ? g * 16 : OOB, WS_MODP + (b * 64 + w * 16) * 4, v);
    } else if (w == 4) {
        const float NI = -__builtin_inff();
        f32x4 mcur, mprev; bool vc[4], vp[4];
#pragma unroll
        for (int r = 0; r < 4; ++r) {
            const int d = (int)tk - (int)(4 * g + r); vc[r] = d >= 0 && !(d & 1); vp[r] = d <= -2 && !(d & 1);
            mcur[r] = vc[r] ? 0.f : NI; mprev[r] = vp[r] ? 0.f : NI;
        }
        const u32x4 M = {(vp[0] ? 0xffffu : 0u) | (vp[1] ? 0xffff0000u : 0u), (vp[2] ? 0xffffu : 0u) | (vp[3] ? 0xffff0000u : 0u),
                         (vc[0] ? 0xffffu : 0u) | (vc[1] ? 0xffff0000u : 0u), (vc[2] ? 0xffffu : 0u) | (vc[3] ? 0xffff0000u : 0u)};
        LD(f32x4, L_MK + l16) = mcur; LD(f32x4, L_MK + 1024 + l16) = mprev; LD(u32x4, L_MK + 2048 + l16) = M;
        LD(u32x4, L_MK + 3072 + l16) = (u32x4){0x3f803f80u, 0x3f803f80u, 0x3f803f80u, 0x3f803f80u};
    }
    STAMP(2);
    qkv_issue(0, 48 * K); qkv_issue(1, 72 * K);
    float t[10][8];
#pragma unroll
    for (int h5 = 0; h5 < 2; ++h5) {
        if (h5 == 0) { VMW(11); } else { VMW(9); }
        float s[5], qq[5];
#pragma unroll
        for (int n = 0; n < 5; ++n) {
            TIE1(xr[h5 * 5 + n]);
            const u32x4 v = xr[h5 * 5 + n];
            s[n] = dot2(v[3], 0x3f803f80u, dot2(v[2], 0x3f803f80u, dot2(v[1], 0x3f803f80u, dot2(v[0], 0x3f803f80u, 0.f))));
            qq[n] = dot2(v[3], v[3], dot2(v[2], v[2], dot2(v[1], v[1], dot2(v[0], v[0], 0.f))));
        }
#define A5(F) F(0) F(1) F(2) F(3) F(4)
#define ST_A(n) "v_add_f32_dpp %" #n ", %" #n ", %" #n " row_half_mirror row_mask:0xf bank_mask:0x5\n\t"
#define ST_C(n) "v_add_f32_dpp %" #n ", %" #n ", %" #n " quad_perm:[1,0,3,2] row_mask:0xf bank_mask:0xf\n\t"
#define ST_D(n) "v_add_f32_dpp %" #n ", %" #n ", %" #n " quad_perm:[2,3,0,1] row_mask:0xf bank_mask:0xf\n\t"
#define ST_B(n, m) "v_add_f32_dpp %" #n ", %" #m ", %" #m " row_half_mirror row_mask:0xf bank_mask:0xa\n\t"
        asm volatile("s_nop 1\n\t" A5(ST_A) ST_B(0, 5) ST_B(1, 6) ST_B(2, 7) ST_B(3, 8) ST_B(4, 9) A5(ST_C) A5(ST_D)
                     : "+v"(s[0]), "+v"(s[1]), "+v"(s[2]), "+v"(s[3]), "+v"(s[4]) : "v"(qq[0]), "v"(qq[1]), "v"(qq[2]), "v"(qq[3]), "v"(qq[4]));
        unsigned sa = L_STAT + r8 * 64 + w * 8 + (c8 >> 2) * 4; PIN(sa);
#pragma unroll
        for (int n = 0; n < 5; ++n) LD(float, sa + (h5 * 5 + n) * 512) = s[n];
        if (h5 == 0) qkv_issue(2, 120 * K); else qkv_issue(3, 96 * K);
    }
    TIE1(ep);
    const unsigned tag = ep + 0x01010101u;
    auto mod_ld = [&](unsigned g0, unsigned g1, u32x4& a, u32x4& c) AI {
        a = ld16c<0>(rw, l16, WS_MODP + g0 * 1024); c = ld16c<0>(rw, l16, WS_MODP + g1 * 1024);
    };
    auto mod_bad = [&](u32x4 a, u32x4 c) AI {
        const unsigned ta = __builtin_amdgcn_perm(__builtin_amdgcn_perm(a[3], a[2], 0x0c0c0400u), __builtin_amdgcn_perm(a[1], a[0], 0x0c0c0400u), 0x05040100u);
        const unsigned tc = __builtin_amdgcn_perm(__builtin_amdgcn_perm(c[3], c[2], 0x0c0c0400u), __builtin_amdgcn_perm(c[1], c[0], 0x0c0c0400u), 0x05040100u);
        return anyl(ta != tag || tc != tag);
    };
    auto mod_fin = [&](unsigned g0, unsigned g1, u32x4 a, u32x4 c, float add1) AI {
        TIE2(a, c);
#pragma nounroll
        for (int it = 0; it < SPIN && mod_bad(a, c); ++it) { mod_ld(g0, g1, a, c); VMW(0); TIE2(a, c); }
        float s[4];
#pragma unroll
        for (int j = 0; j < 4; ++j) s[j] = sw16add(U2F(a[j] & 0xffffff00u), U2F(c[j] & 0xffffff00u));
        const unsigned r = pk2(sw32add(s[0], s[2]), sw32add(s[1], s[3]));
        const float ad = (g & 1) ? add1 : 0.f;
        return pk2(lo16(r) + ad, hi16(r) + ad);
    };
    auto unpack = [&](int n0, int n1) AI {
#pragma unroll
        for (int n = n0; n < n1; ++n)
#pragma unroll
            for (int j = 0; j < 4; ++j) { t[n][2 * j] = lo16(xr[n][j]); t[n][2 * j + 1] = hi16(xr[n][j]); PIN(t[n][2 * j]); PIN(t[n][2 * j + 1]); }
    };
    STAMP(3);
    BAR();
    STAMP(4);
    if (w == 4 || w == 5) {
        const unsigned row = w == 4 ? lane : 64 + tk;
        const f32x4 a = LD(f32x4, L_STAT + row * 64) + LD(f32x4, L_STAT + row * 64 + 16) + LD(f32x4, L_STAT + row * 64 + 32) + LD(f32x4, L_STAT + row * 64 + 48);
        const float mu = (a[0] + a[2]) * (1.f / 512.f);
        const float var = __builtin_fmaxf((a[1] + a[3]) * (1.f / 512.f) - mu * mu, 0.f);
        const float rs = __builtin_amdgcn_rsqf(var + 1e-5f);
        LD(f32x2, L_RST + row * 8) = (f32x2){rs, -mu * rs};
    }
    qkv_issue(4, 0);
    unpack(0, 5);
    qkv_issue(5, 24 * K);
    unpack(5, 10);
    u32x4 ma, mc;
    mod_ld(w, 8 + w, ma, mc);
    VMW(11); BAR();
    STAMP(5);
    bf16x8 Wq[8][3];
    const unsigned trq = kh * 12288 + g * 3072 + q * 128 + (((2 * hd) ^ (fl << 1)) + (p >> 1)) * 16 + (p & 1) * 8;
    unsigned trqh = trq + 72 * K; PIN(trqh);
    auto qkv_read = [&](int r, unsigned base, unsigned slot) AI {
#pragma unroll
        for (int m = 0; m < 3; ++m) Wq[r][m] = TRF(base + slot + m * 1024, 512);
    };
    unsigned ra = L_RST + r8 * 8; PIN(ra);
    auto nrm = [&](int n) AI {
        const f32x2 st = LD(f32x2, ra + n * 64);
#pragma unroll
        for (int j = 0; j < 8; ++j) { t[n][j] = __builtin_fmaf(t[n][j], st.x, st.y); PIN(t[n][j]); }
    };
    qkv_read(0, trq, 48 * K); qkv_read(1, trqh, 0); qkv_read(2, trqh, 48 * K);
    nrm(0); nrm(1);
    BAR();
    STAMP(6);
    qkv_issue(6, 48 * K);
    nrm(2); nrm(3); nrm(4); nrm(5);
    qkv_issue(7, 72 * K);
    nrm(6); nrm(7); nrm(8); nrm(9);
    const unsigned tile0 = cg * 11 > 75 ? 75 : cg * 11;
    const unsigned hrot = (hd - rot) & 3;
    STAMP(7);
    auto body = [&](auto KHC) AI {
    constexpr bool A = decltype(KHC)::v == 0, B = !A;
    constexpr unsigned KH = decltype(KHC)::v;
    if (B) __builtin_amdgcn_s_setprio(1);
    bf16x8 Wo[4][4], W1[8][3], W2[6][4];
    unsigned v1o[3];
    const unsigned f1 = hd == 3 ? 8 : 3 * hd;
    auto v1o_mk = [&]() AI {
#pragma unroll
        for (int j = 0; j < 3; ++j) { const unsigned ch = (f1 + j) * 64 + lane, r = ch / 22; v1o[j] = r * 2752 + (ch - r * 22) * 16 - j * 1024; PIN(v1o[j]); }
    };
    auto w1_issue = [&](int h2, int r, unsigned slot) AI {
        dma3v(r1, LB + slot + f1 * 1024, v1o[0], v1o[1], v1o[2], (h2 * 8 + ((r + 2 * rot) & 7)) * (32 * 2752) + tile0 * 32);
    };
    auto w1_one = [&](int h2, int r, unsigned slot, auto jc) AI {
        CI(j, jc);
        dma1o<j * 1024>(r1, LB + slot + f1 * 1024, v1o[j], (h2 * 8 + ((r + 2 * rot) & 7)) * (32 * 2752) + tile0 * 32);
    };
    auto wo_one = [&](int r, auto jc) AI {
        CI(j, jc);
        dma1o<j * 1024>(ro, LB + r * 16384 + hd * 4096, (j & 1) ? vo1k1 : vo1k0, (hd * 4 + r) * 32768 + cg * 128 + j * 7168);
    };
    auto row_read = [&](bf16x8* W, unsigned base) AI {
        W[0] = TRF(base + tre, 512); W[1] = TRF(base + tro, 512); W[2] = TRF(base + (tre ^ 64), 512); W[3] = TRF(base + (tro ^ 64), 512);
    };
    unsigned tr1;
    auto w1_read = [&](int r, unsigned slot) AI {
        unsigned ba = tr1; if (slot >= 64 * K) { ba += 64 * K; PIN(ba); slot -= 64 * K; }
#pragma unroll
        for (int m = 0; m < 3; ++m) W1[r][m] = TRF(ba + slot + m * 32, 5632);
    };
    unsigned aw0 = L_H + ((2 * (KH * 4 + hrot) + (c8 >> 2)) * 64 + (c8 & 3) * 16 + (r8 ^ ((c8 & 3) | ((c8 >> 2) * 12)))) * 16, aw1 = aw0 ^ 128;
    unsigned ar0 = L_H + (KH * 512 + g * 16 + (tk ^ g)) * 16, ar1 = L_H + (KH * 512 + 64 + g * 16 + (tk ^ (g | 12))) * 16;
    unsigned xp = L_X + hd * 3072 + l16;
    PIN(aw0); PIN(aw1); PIN(ar0); PIN(ar1); PIN(xp);
    VMW(6);
    STAMP(8);
    f32x4 A0, A1, S0, S1;
    {
        const unsigned r = mod_fin(w, 8 + w, ma, mc, 1.f);
        LD(f32x2, L_AS + w * 512 + (g & 1) * 256 + tk * 16 + (g >> 1) * 8) = (f32x2){lo16(r), hi16(r)};
        unsigned aa = L_AS + w * 512 + c8 * 32; PIN(aa);
        S0 = LD(f32x4, aa); S1 = LD(f32x4, aa + 16); A0 = LD(f32x4, aa + 256); A1 = LD(f32x4, aa + 272);
    }
    STAMP(9);
    u32x4 hw;
    auto norm1 = [&](auto ic) AI {
        constexpr int n = decltype(ic)::v >> 2, j = decltype(ic)::v & 3;
        const f32x4 Av = j < 2 ? A0 : A1, Sv = j < 2 ? S0 : S1;
        hw[j] = pk2(__builtin_fmaf(t[n][2 * j], Av[(2 * j) & 3], Sv[(2 * j) & 3]), __builtin_fmaf(t[n][2 * j + 1], Av[(2 * j + 1) & 3], Sv[(2 * j + 1) & 3]));
        IF (j == 3) LD(u32x4, ((n >> 1) & 1) * 16384 + ((n & 1) ? aw1 : aw0)) = hw;
    };
    sfor<0, 8>(norm1);
    BAR();
    STAMP(10);
    bf16x8 hf[8];
    auto h_rd = [&](int s, unsigned base) AI { hf[s] = LD(bf16x8, base + (s >> 1) * 2048 + ((s & 1) ? ar1 : ar0)); };

    f32x4 aq, ak, av, oq, ok, ov;
    u32x2 kc, vc, kp, vp;
    f32x4 pq_, pk_, pv_, mcur, mprev, sp, sc, sm, ao; u32x4 M, ones, P; unsigned q0, q1, mkp; float t0, t1, mx, inv;
    auto attn = [&](auto Tc, auto ic) AI {
        CI(T, Tc); CI(i, ic);
        const unsigned xb = xp + (T & 1) * 12288;
        constexpr float C = 0.25f * 1.44269504089f;
        if (T == 0) {
            if (i == 0) { pk_ = LD(f32x4, xb + 1024); pv_ = LD(f32x4, xb + 2048); }
            if (i == 5) kc = (u32x2){pk2(ok[0] + pk_[0], ok[1] + pk_[1]), pk2(ok[2] + pk_[2], ok[3] + pk_[3])};
            if (i == 7) vc = (u32x2){pk2(ov[0] + pv_[0], ov[1] + pv_[1]), pk2(ov[2] + pv_[2], ov[3] + pv_[3])};
            return;
        }
        if (i == 0) { pq_ = LD(f32x4, xb); mprev = LD(f32x4, mkp + 1024); pk_ = LD(f32x4, xb + 1024); mcur = LD(f32x4, mkp); pv_ = LD(f32x4, xb + 2048); kp = kc; vp = vc; }
        if (i == 4) {
            if (T == 1) { const float ni = rg == 0 ? -__builtin_inff() : 0.f; mprev += ni; }
            oq += pq_; q0 = pk2(oq[0], oq[1]); q1 = pk2(oq[2], oq[3]); sp = mfma16u(kp[0], kp[1], q0, q1, mprev);
        }
        if (i == 5) { ok += pk_; kc = (u32x2){pk2(ok[0], ok[1]), pk2(ok[2], ok[3])}; sc = mfma16u(kc[0], kc[1], q0, q1, mcur); M = LD(u32x4, mkp + 2048); ones = LD(u32x4, mkp + 3072); }
        if (i == 6) { ov += pv_; vc = (u32x2){pk2(ov[0], ov[1]), pk2(ov[2], ov[3])}; }
        if (i == 7) { t0 = fmx(fmx(fmx(sp[0], sp[1]), sc[0]), sc[1]); t1 = fmx(fmx(fmx(sp[2], sp[3]), sc[2]), sc[3]); }
        if (i == 8) mx = xmax16(fmx(t0, t1));
        if (i == 9) mx = xmax32(mx) * C;
        if (i == 10) { t0 = __builtin_amdgcn_exp2f(__builtin_fmaf(t0, C, -mx)); t1 = __builtin_amdgcn_exp2f(__builtin_fmaf(t1, C, -mx)); }
        if (i == 11) { q0 = pk2(t0, t0); q1 = pk2(t1, t1); P = (u32x4){q0 & M[0], q1 & M[1], q0 & M[2], q1 & M[3]}; }
        if (i == 12) { sm = mfma32u(ones, P, (f32x4){0, 0, 0, 0}); ao = mfma32u((u32x4){vp[0], vp[1], vc[0], vc[1]}, P, (f32x4){0, 0, 0, 0}); }
        if (i == 14) inv = __builtin_amdgcn_rcpf(sm[0]);
        if (i == 15) { ao *= inv; q0 = pk2(ao[0], ao[1]); q1 = pk2(ao[2], ao[3]); }
        if (i == 16) {
            auto ra_ = __builtin_amdgcn_permlane16_swap(q0, q0, false, false);
            auto rb_ = __builtin_amdgcn_permlane16_swap(q1, q1, false, false);
            st16s(rw, vos, WS_O + ((rg * 4 + T - 1) * 16 + cg * 2) * 1024 + hd * 512, (u32x4){ra_[0], rb_[0], ra_[1], rb_[1]});
        }
    };
    const u32x4 ff = {~0u, ~0u, ~0u, ~0u};
    auto clr = [&](int n) AI {
        const unsigned vw = n < 6 ? hd : hd + 4; const int i = n < 6 ? n : n - 6;
        const unsigned ub = WS_U + (rg * 4 + (vw >> 1)) * 44032 + tile0 * 512 + (vw & 1) * 3072;
        const unsigned lo32 = lane < 32 ? 0u : OOB;
        if (i == 0) st16s(rw, l16, WS_O + ((rg * 4 + (vw >> 1)) * 16 + cg * 2 + (vw & 1)) * 1024, ff);
        if (i == 1) st16s(rw, vy, WS_Y + (rg * 64 + vw * 8) * 1024 + cg * 128, ff);
        if (i == 2) st16s(rw, l16, (cg < 7 || (vw & 1)) ? ub : OOB, ff);
        if (i == 3) st16s(rw, l16, ub + 1024, ff);
        if (i == 4) st16s(rw, l16 | ((vw & 1) ? lo32 : 0u), ub + 2048, ff);
        if (i == 5) st16s(rw, vy | lo32, vw == 0 ? WS_ST + (rg * 32 + cg) * 128 : OOB, ff);
    };
    u32x4 m2a, m2c, m3a, m3c, m4a, m4c; unsigned flg;
    u32x4 of[4];
    auto o_ld = [&](int tt) AI {
        const unsigned so = WS_O + ((rg * 4 + tt) * 16 + 4 * hd) * 1024;
        of[0] = ld16c<0>(rw, l16, so); of[1] = ld16c<1024>(rw, l16, so); of[2] = ld16c<2048>(rw, l16, so); of[3] = ld16c<3072>(rw, l16, so);
    };
    auto cbw = [&]() AI { return (g & 1) * 1024 + (tk >> 3) * 64 + (tk & 3) * 16 + ((tk >> 2) & 1) * 8 + (g >> 1) * 4; };
    u32x2 xres[4]; unsigned fvo;
    auto qside = [&](auto Tc, auto ic) AI {
        CI(T, Tc); CI(i, ic);
        constexpr unsigned HN = ((T + 1) & 1) * 16384;
        constexpr int BI = T == 0 ? 7 : 4;
        IF (T == 0 && i < 2) { norm1(IC<8 + 2 * i>{}); norm1(IC<9 + 2 * i>{}); }
        IF (T == 0 && i >= 2 && i < 14) norm1(IC<10 + i>{});
        IF (T == 1 && i >= 6 && i < 22 && !(i & 1)) norm1(IC<24 + (i - 6) / 2>{});
        IF (T == 2 && i < 3) norm1(IC<32 + i>{});
        IF (T == 2 && i >= 5 && i < 8) norm1(IC<30 + i>{});
        IF (T == 2 && A && (i == 8 || i == 9)) norm1(IC<30 + i>{});
        IF (T == 2 && B && (i == 21 || i == 22)) norm1(IC<17 + i>{});
        if (A) {
            const unsigned xb = xp + ((T - 1) & 1) * 12288;
            if (T > 1 && i == 0) LD(f32x4, xb) = oq;
            if (T > 0 && i == 1) LD(f32x4, xb + 1024) = ok;
            if (T > 0 && i == 2) LD(f32x4, xb + 2048) = ov;
            IF (T == 1 && i % 3 == 1) wo_one(i / 12, IC<(i / 3) & 3>{});
            IF (T == 2 && i < 16 && !(i & 1)) wo_one(2 + i / 8, IC<(i / 2) & 3>{});
            IF (T == 2 && i >= 16 && i < 22 && !(i & 1)) w1_one(1, 0, Z5, IC<(i - 16) / 2>{});
            if (T == 2 && i == 3) { VMW(6); row_read(Wo[0], 0); }
            if (T == 2 && i == 11) { v1o_mk(); }
            if (T == 2 && i == 13) { VMW(7); row_read(Wo[1], 16384); }
            IF (T == 3 && i < 18 && !(i & 1)) w1_one(i < 12 ? 1 : 0, i < 6 ? 1 : i < 12 ? 2 : 0, i < 6 ? Z6 : i < 12 ? Z7 : Z0, IC<(i / 2) % 3>{});
            if (T == 3 && i == 1) { VMW(8); row_read(Wo[2], 32768); }
            if (T == 3 && i == 9) { VMW(8); row_read(Wo[3], 49152); }
            if (T == 3 && i == 18) mod_ld(24 + hd, 32 + hd, m2a, m2c);
            if (T == 3 && i == 21) mod_ld(16 + cg, 40 + cg, m3a, m3c);
            IF (T == 4 && i < 6 && !(i & 1)) w1_one(0, 1, Z1, IC<i / 2>{});
            if (T == 4 && i == 4) VMW(7);
            if (T == 4 && i == 5) { mod_ld(28 + hd, 36 + hd, m4a, m4c); flg = ld4c(rw, (lane & 31) * 4, WS_FLAG + rg * 128); }
            IF (T == 4 && i >= 7 && i < 19 && (i & 1)) w1_one(0, i < 13 ? 2 : 3, i < 13 ? Z2 : Z3, IC<((i - 7) / 2) % 3>{});
            if (T == 4 && i == 6) w1_read(0, Z0);
            if (T == 4 && i == 8) { VMW(7); LD(unsigned, L_CB + hrot * 128 + cbw()) = mod_fin(24 + hd, 32 + hd, m2a, m2c, 1.f); }
            if (T == 4 && i == 14) { const unsigned r = mod_fin(16 + cg, 40 + cg, m3a, m3c, 0.f); LD(unsigned, L_GT + cbw() - (g & 1) * 896) = r; }
            if (T == 4 && i == 21) {
                VMW(6); TIE1(flg);
#pragma nounroll
                for (int it = 0; it < SPIN && anyl(flg != tag); ++it) { flg = ld4c(rw, (lane & 31) * 4, WS_FLAG + rg * 128); VMW(0); TIE1(flg); }
                o_ld(0);
            }
            if (T == 0 && i == 7) VMW(0);
        } else {
            if (T == 0 && !(i & 1)) clr(i / 2);
            if (T == 1 && i < 6 && !(i & 1)) clr(8 + i / 2);
            if (T == 0 && i == 7) VMW(4);
            if (T == 2 && i == 3) { VMW(0); st4s(rw, lane == 0 ? 0u : OOB, WS_FLAG + rg * 128 + cg * 16 + hd * 4, tag); }
            IF (T > 0 && i > 4) attn(IC<T - 1>{}, IC<i - 4>{});
            if (T == 4 && i == 6) {
                fvo = (hd * 4 + g) * 1024 + ((lane >> 3) & 1) * 64 + (lane & 1) * 32 + ((lane >> 1) & 3) * 8; PIN(fvo);
#pragma unroll
                for (int tt = 0; tt < 4; ++tt) xres[tt] = ld8(rx, fvo, (rg * 64 + tt * 16) * 1024 + cg * 128);
            }
            if (T == 4 && i == 13) w1_read(0, Z5);
            if (T == 4 && i == 17) w1_read(1, Z6);
            if (T == 4 && i == 21) w1_read(2, Z7);
        }
        if (i == BI) {
            STAMP(40 + 2 * T);
            BAR();
            STAMP(41 + 2 * T);
            IF (B && T > 0) attn(IC<T - 1>{}, IC<0>{});
        }
        IF (T == 0) {
            if (i == 0) { qkv_read(4, trq, 0); }
            if (i == 2) { qkv_read(5, trq, 24 * K); }
            if (i == 7) { qkv_read(6, trq, 48 * K); }
            if (i == 8) { qkv_read(7, trqh, 0); h_rd(0, HN); h_rd(1, HN); }
            if (i == 9) { h_rd(2, HN); h_rd(3, HN); h_rd(4, HN); }
            if (i == 11) h_rd(5, HN);
            if (i == 13) h_rd(6, HN);
            if (i == 15) h_rd(7, HN);
        }
        IF (T > 0 && T < 4) {
            if (i == 5) h_rd(0, HN);
            if (i >= 5 && i % 3 == 2) h_rd(i / 3, HN);
        }
        SB();
    };
    if (B) { mkp = L_MK + l16; PIN(mkp); PIN(vos); }
    tr1 = (4 * g + q) * 352 + hd * 96 + p * 8; PIN(tr1);
    h_rd(0, 0); h_rd(1, 0); h_rd(2, 0); h_rd(3, 0); qkv_read(3, trqh, 24 * K); h_rd(4, 0); h_rd(5, 0); h_rd(6, 0); h_rd(7, 0);
    sfor<0, 5>([&](auto Tc) AI {
        CI(T, Tc);
        oq = aq; ok = ak; ov = av;
        aq = (f32x4){0, 0, 0, 0}; ak = aq; av = aq;
        SB();
        sfor<0, 8>([&](auto sc_) AI {
            CI(s, sc_);
            IF (T > 0) { aq = mfma32(Wq[s][0], hf[s], aq); qside(Tc, IC<3 * s>{}); }
            ak = mfma32(Wq[s][1], hf[s], ak); qside(Tc, IC<(T > 0 ? 3 * s + 1 : 2 * s)>{});
            av = mfma32(hf[s], Wq[s][2], av); qside(Tc, IC<(T > 0 ? 3 * s + 2 : 2 * s + 1)>{});
        });
        STAMP(11 + T);
    });
    if (A) { LD(f32x4, xp) = aq; LD(f32x4, xp + 1024) = ak; LD(f32x4, xp + 2048) = av; }
    oq = aq; ok = ak; ov = av;
    BAR();

    STAMP(56);
    if (B) {
        sfor<0, 17>([&](auto ic) AI { attn(IC<4>{}, ic); });
        SB();
    } else {
        w1_issue(1, 3, Z5); w1_issue(1, 4, Z6); w1_issue(0, 4, Z4);
        w1_read(1, Z1);
        LD(unsigned, L_CB + 512 + hrot * 128 + cbw()) = mod_fin(28 + hd, 36 + hd, m4a, m4c, 1.f);
    }
    STAMP(50);
    RELANE();
    const unsigned fb = (lane >> 3) & 1, fg = (lane >> 1) & 3, fe = lane & 1, ftok = hd * 4 + g;
    unsigned pw0 = w * 4096 + tk * 256 + (g ^ (tk & 7)) * 16;
    unsigned pr = ftok * 256 + ((fb * 8 + fe * 4 + fg) ^ (ftok & 7)) * 16;
    auto w2_issue = [&](unsigned vw, int s, unsigned slot) AI {
        const unsigned ks = vw * 5 + (vw < 3 ? vw : 3) + s;
        dma4(r2, LB + slot + vw * 4096, vo1k0, vo1k1, ks * 32768 + cg * 128);
    };
    auto w2_one = [&](unsigned vw, int s, unsigned slot, auto jc) AI {
        CI(j, jc);
        const unsigned ks = vw * 5 + (vw < 3 ? vw : 3) + s;
        dma1o<j * 1024>(r2, LB + slot + vw * 4096, (j & 1) ? vo1k1 : vo1k0, ks * 32768 + cg * 128 + j * 7168);
    };
    u32x2 x1p[4];
    f32x4 sacc[3] = {{0, 0, 0, 0}, {0, 0, 0, 0}, {0, 0, 0, 0}};
    auto s1_part = [&](int s0, int s1) AI {
        unsigned ca = L_CB + (tk & 1) * 1024 + KH * 512 + g * 16; PIN(ca);
        bf16x8 yf[8];
#pragma unroll
        for (int s = s0; s < s1; ++s) yf[s] = LD(bf16x8, ca + s * 64);
#pragma unroll
        for (int s = s0; s < s1; ++s)
#pragma unroll
            for (int m = 0; m < 3; ++m) sacc[m] = mfma32(W1[s][m], yf[s], sacc[m]);
        SB();
    };
    u32x4 yv[4], sv;
    auto y_ld = [&](int tt) AI {
        const unsigned so = WS_Y + (rg * 4 + tt) * 16384 + hd * 128;
        yv[0] = ld16c<0>(rw, vy, so); yv[1] = ld16c<0>(rw, vy, so + 8192); yv[2] = ld16c<512>(rw, vy, so); yv[3] = ld16c<512>(rw, vy, so + 8192);
        sv = ld16c<0>(rw, l16, hd == (unsigned)tt ? WS_ST + (rg * 4 + tt) * 1024 : OOB);
    };
    aw0 += L_ACT - L_H; aw1 += L_ACT - L_H; ar0 += L_ACT - L_H; ar1 += L_ACT - L_H; PIN(ar0); PIN(ar1);
    if (A) { PIN(aw0); PIN(aw1); }
    auto y_put = [&](int tt) AI {
        TIE2(yv[0], yv[1]); TIE2(yv[2], yv[3]); TIE1(sv);
#pragma nounroll
        for (int it = 0; it < SPIN && anyl(umax4(sv, umax4(yv[3], umax4(yv[2], umax4(yv[1], umax4(yv[0], 0))))) == ~0u); ++it) {
            y_ld(tt); VMW(0); TIE2(yv[0], yv[1]); TIE2(yv[2], yv[3]); TIE1(sv);
        }
        LD(u32x4, (tt & 1) * 16384 + aw0) = yv[0]; LD(u32x4, (tt & 1) * 16384 + aw1) = yv[1];
        LD(u32x4, (tt & 1) * 16384 + 8192 + aw0) = yv[2]; LD(u32x4, (tt & 1) * 16384 + 8192 + aw1) = yv[3];
    };
    auto y_stat = [&](int tt) AI {
        if (hd == (unsigned)tt) {
            float s = sw16add(U2F(sv[0]), U2F(sv[2])), qq = sw16add(U2F(sv[1]), U2F(sv[3]));
            s += dpp<0x128>(s); qq += dpp<0x128>(qq);
            s = sw32add(s, s); qq = sw32add(qq, qq);
            const float mu = s * (1.f / 512.f);
            const float var = __builtin_fmaxf(qq * (1.f / 512.f) - mu * mu, 0.f);
            LD(f32x2, L_SST + tt * 128 + (c8 * 2 + (g & 1)) * 8) = (f32x2){-mu, -1.44269504089f * __builtin_amdgcn_rsqf(var + 1e-5f)};
        }
    };
    if (A) {
        unsigned pa0 = L_PW + pw0, pa1 = pa0 ^ 64; PIN(pa0); PIN(pa1);
        auto wo_step = [&](int tt) AI {
            TIE2(of[0], of[1]); TIE2(of[2], of[3]);
#pragma nounroll
            for (int it = 0; it < SPIN && anyl(umax4(of[3], umax4(of[2], umax4(of[1], umax4(of[0], 0)))) == ~0u); ++it) { o_ld(tt); VMW(0); TIE2(of[0], of[1]); TIE2(of[2], of[3]); }
            STAMP(16 + tt * 2);
            if (tt == 3) y_ld(0);
            f32x4 acc[4] = {{0, 0, 0, 0}, {0, 0, 0, 0}, {0, 0, 0, 0}, {0, 0, 0, 0}};
#pragma unroll
            for (int c = 0; c < 4; ++c) {
#pragma unroll
                for (int j = 0; j < 4; ++j) acc[c] = mfma32(Wo[j][c], __builtin_bit_cast(bf16x8, of[j]), acc[c]);
                if (c > 0) LD(f32x4, (tt & 1) * PWD + ((c - 1) >> 1) * 128 + (((c - 1) & 1) ? pa1 : pa0)) = acc[c - 1];
                SB();
            }
            if (tt < 3) o_ld(tt + 1);
            LD(f32x4, (tt & 1) * PWD + 128 + pa1) = acc[3];
        };
        VMW(9); wo_step(0);
        VMW(4); BAR();
        STAMP(17);
        w1_read(2, Z2); w1_read(3, Z3); w1_read(4, Z4);
        s1_part(0, 2);
        VMW(0); wo_step(1);
        XS(2);
        BAR();
        STAMP(19);
        s1_part(2, 5);
        XS(3);
        VMW(0); XS(4); wo_step(2);
        XS(5);
        BAR();
        STAMP(21);
        w1_read(5, Z8); w1_read(6, Z9);
        s1_part(5, 7);
        XS(6);
        VMW(0); XS(7); wo_step(3);
        XS(8);
        BAR();
        STAMP(23);
        w1_read(7, Z3);
        VMW(0); y_put(0);
        STAMP(51);
        y_stat(0);
        y_ld(1);
        s1_part(7, 8);
#pragma unroll
        for (int m = 0; m < 3; ++m) LD(f32x4, L_S1 + (tk & 1) * 768 + (hd * 48 + m * 16 + g * 4) * 4) = sacc[m];
        XS(57);
    } else {
        unsigned prw = L_PW + pr; PIN(prw);
        f32x4 ga, A2;
        auto fin_wo = [&](int tt) AI {
            f32x4 pp[4];
#pragma unroll
            for (int ww = 0; ww < 4; ++ww) pp[ww] = LD(f32x4, prw + (tt & 1) * PWD + ww * 4096);
            if (tt == 0) {
                const u32x2 gv = LD(u32x2, L_GT + fb * 64 + fg * 16 + fe * 8);
                const u32x2 av2 = LD(u32x2, L_CB + 1024 + ((cg & 4) + ((cg - rot) & 3)) * 128 + fb * 64 + fg * 16 + fe * 8);
                ga = (f32x4){lo16(gv[0]), hi16(gv[0]), lo16(gv[1]), hi16(gv[1])};
                A2 = (f32x4){lo16(av2[0]), hi16(av2[0]), lo16(av2[1]), hi16(av2[1])};
            }
            const u32x2 xr2 = xres[tt];
            const f32x4 xv = {lo16(xr2[0]), hi16(xr2[0]), lo16(xr2[1]), hi16(xr2[1])};
            const f32x4 sum = (pp[0] + pp[1]) + (pp[2] + pp[3]);
            const f32x4 v = ga * sum + xv;
            const f32x4 xx = v * A2;
            const unsigned y0 = pk2(xx[0], xx[1]), y1 = pk2(xx[2], xx[3]);
            const u32x4 yy = {y0, y1, dppu<0xF5>(y0), dppu<0xF5>(y1)};
            st16s(rw, fe ? OOB : ftok * 1024 + tk * 8, WS_Y + (rg * 4 + tt) * 16384 + cg * 128, yy);
            const u32x2 x1 = {pk2(v[0], v[1]), pk2(v[2], v[3])};
            x1p[tt] = x1;
            float s = dot2(x1[1], 0x3f803f80u, dot2(x1[0], 0x3f803f80u, 0.f));
            const float qq = dot2(x1[1], x1[1], dot2(x1[0], x1[0], 0.f));
            asm volatile("s_nop 1\n\tv_add_f32_dpp %0, %0, %0 row_ror:8 row_mask:0xf bank_mask:0x3\n\tv_add_f32_dpp %0, %1, %1 row_ror:8 row_mask:0xf bank_mask:0xc\n\ts_nop 1" : "+v"(s) : "v"(qq));
            s += dpp<0x141>(s); s += dpp<0xB1>(s); s += dpp<0x4E>(s);
            st4s(rw, c8 ? OOB : ftok * 8 + fb * 4, WS_ST + ((rg * 4 + tt) * 8 + cg) * 128, F2U(s));
            SB();
        };
        v1o_mk();
        VMW(2); TIE2(xres[0], xres[1]); TIE2(xres[2], xres[3]);
        BAR();
        STAMP(17);
        fin_wo(0);
        STAMP(18);
        w1_issue(0, 5, Z8); w1_issue(0, 6, Z9); w1_issue(1, 5, Z7);
        XS(1);
        w1_read(3, Z5); w1_read(4, Z6);
        XS(2);
        BAR();
        STAMP(19);
        fin_wo(1);
        STAMP(20);
        w1_issue(0, 7, Z3); w1_issue(1, 6, Z4); w1_issue(1, 7, Z5);
        XS(3);
        s1_part(0, 3);
        XS(4);
        VMW(11);
        XS(5);
        BAR();
        STAMP(21);
        fin_wo(2);
        STAMP(22);
        w2_issue(hd, 0, 0); w2_issue(hd + 4, 0, 0);
        XS(6);
        w1_read(5, Z7);
        s1_part(3, 6);
        XS(7);
        VMW(10);
        XS(8);
        BAR();
        STAMP(23);
        fin_wo(3);
        STAMP(51);
        w1_read(6, Z4); w1_read(7, Z5);
        w2_issue(hd + 4, 4, 64 * K);
        s1_part(6, 8);
        XS(9);
        VMW(6);
        XS(57);
    }
    BAR();
    STAMP(24);

    h_rd(0, 0); h_rd(1, 0); h_rd(2, 0); h_rd(3, 0); h_rd(4, 0); h_rd(5, 0); h_rd(6, 0); h_rd(7, 0);
    unsigned xp1 = L_XC1 + hd * 3072 + l16; PIN(xp1);
    unsigned s1a = L_S1 + hd * 192 + g * 16;
    unsigned vus0 = (g >> 1) * 256 + tk * 16 + (g & 1) * 512;
    unsigned ssa = L_SST + tk * 8;
    if (B) { PIN(s1a); PIN(vus0); PIN(ssa); PIN(vos); }
    const unsigned ks0 = w * 5 + (w < 3 ? w : 3);
    u32x4 uf[2][6];
    auto u_ld = [&](int tt) AI {
        const unsigned so = WS_U + ((rg * 4 + tt) * 43 + ks0) * 1024;
        u32x4* u = uf[tt & 1];
        u[0] = ld16c<0>(rw, l16, so); u[1] = ld16c<1024>(rw, l16, so); u[2] = ld16c<2048>(rw, l16, so); u[3] = ld16c<3072>(rw, l16, so);
        u[4] = ld16c<0>(rw, l16, so + 4096);
        if (A) u[5] = ld16c<0>(rw, l16, w < 3 ? so + 5120 : OOB);
    };
    f32x4 acc[3], oa[3], pa[3], t1v[3], s1v[3], ev[3]; f32x2 st; unsigned ua[3], ub[3];
    auto silu = [&](auto tc, auto ic) AI {
        CI(tt, tc); CI(i, ic);
        const unsigned xb = xp1 + (tt & 1) * 12288;
        auto Lm = [&](int m) AI { s1v[m] = LD(f32x4, s1a + 768 + m * 64); t1v[m] = LD(f32x4, s1a + m * 64); };
        auto Pm = [&](int m) AI {
#pragma unroll
            for (int r = 0; r < 4; ++r) oa[m][r] = __builtin_fmaf(st.y, __builtin_fmaf(st.x, s1v[m][r], oa[m][r]), t1v[m][r]);
        };
        auto Zm = [&](int m) AI {
#pragma unroll
            for (int r = 0; r < 4; ++r) oa[m][r] = __builtin_fmaf(st.y, pa[m][r], oa[m][r]);
        };
        auto Em = [&](int m) AI {
#pragma unroll
            for (int r = 0; r < 4; ++r) ev[m][r] = __builtin_amdgcn_exp2f(oa[m][r]);
        };
        auto Cm = [&](int m) AI {
#pragma unroll
            for (int r = 0; r < 4; ++r) ev[m][r] = __builtin_amdgcn_rcpf(ev[m][r]);
        };
        auto Fm = [&](int m) AI { oa[m] *= ev[m]; ua[m] = pk2(oa[m][0], oa[m][1]); ub[m] = pk2(oa[m][2], oa[m][3]); };
        if (i == 0) { st = LD(f32x2, ssa + tt * 128); Lm(0); Lm(1); }
        if (i == 3) { Pm(0); }
        if (i == 4) { pa[0] = LD(f32x4, xb); pa[1] = LD(f32x4, xb + 1024); Lm(2); }
        if (i == 5) Pm(1);
        if (i == 6) pa[2] = LD(f32x4, xb + 2048);
        if (i == 7) Zm(0);
        if (i == 8) Em(0);
        if (i == 9) { Zm(1); ev[0] += 1.f; }
        if (i == 10) Cm(0);
        if (i == 11) Em(1);
        if (i == 12) { Fm(0); ev[1] += 1.f; }
        if (i == 13) Cm(1);
        if (i == 14) Pm(2);
        if (i == 15) Fm(1);
        const unsigned so = WS_U + (rg * 4 + tt) * 44032 + (tile0 + hd * 3) * 512;
        if (i == 16) {
            auto ra_ = __builtin_amdgcn_permlane16_swap(ua[0], ua[1], false, false);
            auto rb_ = __builtin_amdgcn_permlane16_swap(ub[0], ub[1], false, false);
            st16s(rw, vus0, (cg < 7 || hd > 0) ? so : OOB, (u32x4){ra_[0], rb_[0], ra_[1], rb_[1]});
            Zm(2);
        }
        if (i == 17) Em(2);
        if (i == 18) ev[2] += 1.f;
        if (i == 19) Cm(2);
        if (i == 20) Fm(2);
        if (i == 21) {
            auto ra_ = __builtin_amdgcn_permlane16_swap(ua[2], ua[2], false, false);
            auto rb_ = __builtin_amdgcn_permlane16_swap(ub[2], ub[2], false, false);
            st16s(rw, vos, hd < 3 ? so + 1024 : OOB, (u32x4){ra_[0], rb_[0], ra_[1], rb_[1]});
        }
    };
    auto fside = [&](auto tc, auto ic) AI {
        CI(tt, tc); CI(i, ic);
        constexpr unsigned AN = ((tt + 1) & 1) * 16384;
        if (tt > 0 && i == 4) { STAMP(57 + tt); BAR(); }
        if (tt == 0 && i == 2) row_read(W2[0], 0);
        if (tt == 1 && i == 22) row_read(W2[1], 32 * K);
        if (B) {
            IF (tt > 0) silu(IC<tt - 1>{}, ic);
            if (tt == 0 && i == 17) VMW(0);
            if (tt == 0 && i == 6) {
                const float sc1 = (tk & 1) ? 1.f : -1.44269504089f;
#pragma unroll
                for (int m = 0; m < 3; ++m) {
                    const unsigned o = L_S1 + (tk & 1) * 768 + (hd * 48 + m * 16 + g * 4) * 4;
                    LD(f32x4, o) = (LD(f32x4, o) + sacc[m]) * sc1;
                }
            }
        } else {
            IF (tt > 0 && i < 3) LD(f32x4, xp1 + ((tt - 1) & 1) * 12288 + i * 1024) = oa[i];
            IF (tt == 0 && i < 16 && !(i & 1)) w2_one(i < 8 ? hd : hd + 4, 1, 32 * K, IC<(i / 2) & 3>{});
            IF (tt == 1 && i < 16 && !(i & 1)) w2_one(i < 8 ? hd : hd + 4, 2, 0, IC<(i / 2) & 3>{});
            IF (tt == 2 && i < 8 && !(i & 1)) w2_one(hd, 5, 64 * K, IC<(i / 2) & 3>{});
            IF (tt == 2 && i >= 8 && i < 16 && !(i & 1)) w2_one(hd + 4, 3, 32 * K, IC<(i / 2) & 3>{});
            IF (tt == 3 && i < 16 && !(i & 1)) w2_one(hd, i < 8 ? 4 : 3, i < 8 ? 32 * K : 0, IC<(i / 2) & 3>{});
            if (tt < 3 && i == 15) { VMW(8); y_put(tt + 1); }
            if (tt < 3 && i == 17) y_stat(tt + 1);
            if (tt < 2 && i == 23) y_ld(tt + 2);
            if (tt == 2 && i == 21) row_read(W2[2], 0);
        }
        if (tt < 3 && i == 18) { STAMP(52 + tt); BAR(); h_rd(0, AN); h_rd(1, AN); h_rd(2, AN); }
        if (tt < 3 && i == 19) { h_rd(3, AN); h_rd(4, AN); h_rd(5, AN); }
        if (tt < 3 && i == 20) h_rd(6, AN);
        if (tt < 3 && i == 23) h_rd(7, AN);
        if (tt == 3 && i == (A ? 20 : 22)) u_ld(0);
        SB();
    };
    sfor<0, 4>([&](auto tc) AI {
        CI(tt, tc);
#pragma unroll
        for (int m = 0; m < 3; ++m) { oa[m] = acc[m]; acc[m] = (f32x4){0, 0, 0, 0}; }
        SB();
        sfor<0, 24>([&](auto ic) AI { CI(i, ic); acc[i % 3] = mfma32(W1[i / 3][i % 3], hf[i / 3], acc[i % 3]); fside(tc, ic); });
        STAMP(25 + tt);
    });

    if (A) {
#pragma unroll
        for (int m = 0; m < 3; ++m) LD(f32x4, xp1 + 12288 + m * 1024) = acc[m];
        VMW(14);
        BAR();
    } else {
#pragma unroll
        for (int m = 0; m < 3; ++m) oa[m] = acc[m];
        sfor<0, 4>([&](auto ic) AI { silu(IC<3>{}, ic); });
        BAR();
        sfor<4, 22>([&](auto ic) AI { silu(IC<3>{}, ic); });
        SB();
    }
    STAMP(29);
    RELANE();
    if (A) { VMW(6); row_read(W2[3], 0); row_read(W2[4], 32 * K); unsigned tb = 64 * K; row_read(W2[5], tb); }
    else { row_read(W2[2], 0); row_read(W2[3], 32 * K); row_read(W2[4], 64 * K); }
    if (B) u_ld(1);
    f32x4 gb;
    if (B) {
        const u32x2 gv = LD(u32x2, L_GT + 128 + fb * 64 + fg * 16 + fe * 8);
        gb = (f32x4){lo16(gv[0]), hi16(gv[0]), lo16(gv[1]), hi16(gv[1])} * (-1.f / 1.44269504089f);
    }
    unsigned pw1 = pw0 ^ 64; PIN(pw0); PIN(pw1); if (B) PIN(pr);
    f32x4 pp[8];
    auto fin_out = [&](auto tc, auto ic) AI {
        CI(tt, tc); CI(i, ic);
        IF (i < 2) {
#pragma unroll
            for (int ww = 4 * i; ww < 4 * i + 4; ++ww) pp[ww] = LD(f32x4, pr + (tt & 1) * 32768 + ww * 4096);
        }
        if (i == 5) pp[0] += pp[1];
        if (i == 6) pp[2] += pp[3];
        if (i == 7) pp[4] += pp[5];
        if (i == 8) pp[6] += pp[7];
        if (i == 9) pp[0] += pp[2];
        if (i == 10) pp[4] += pp[6];
        const u32x2 xp2 = x1p[tt];
        if (i == 11) { pp[0] += pp[4]; }
        if (i == 12) pp[1] = (f32x4){lo16(xp2[0]), hi16(xp2[0]), lo16(xp2[1]), hi16(xp2[1])};
        if (i == 13) pp[0] = gb * pp[0] + pp[1];
        if (i == 14) __builtin_amdgcn_raw_buffer_store_b64((u32x2){pk2(pp[0][0], pp[0][1]), pk2(pp[0][2], pp[0][3])}, rout, (int)fvo, (int)((rg * 4 + tt) * 16384 + cg * 128), 0);
    };
    sfor<0, 4>([&](auto tc) AI {
        CI(tt, tc);
        u32x4* u = uf[tt & 1];
        IF (B && tt > 0) { fin_out(IC<tt - 1>{}, IC<0>{}); fin_out(IC<tt - 1>{}, IC<1>{}); }
        VMW(A ? (tt == 1 || tt == 2 ? 6 : 0) : (tt == 0 ? 7 : tt == 1 ? 5 : tt == 2 ? 6 : 1));
        TIE2(u[0], u[1]); TIE2(u[2], u[3]); TIE1(u[4]); if (A) TIE1(u[5]);
#pragma nounroll
        for (int it = 0; it < SPIN && anyl(umax4(u[4], umax4(u[3], umax4(u[2], umax4(u[1], umax4(u[0], A ? umax4(u[5], 0) : 0))))) == ~0u); ++it) {
            u_ld(tt); VMW(0); TIE2(u[0], u[1]); TIE2(u[2], u[3]); TIE1(u[4]); if (A) TIE1(u[5]);
        }
        STAMP(30 + tt * 2);
        if (A && tt == 0) u_ld(1);
        f32x4 a2[4] = {{0, 0, 0, 0}, {0, 0, 0, 0}, {0, 0, 0, 0}, {0, 0, 0, 0}};
        if (A && w < 3) {
#pragma unroll
            for (int c = 0; c < 4; ++c) a2[c] = mfma32(W2[5][c], __builtin_bit_cast(bf16x8, u[5]), a2[c]);
        }
        SB();
        sfor<0, 20>([&](auto ic) AI {
            CI(i, ic); constexpr int s = i >> 2, c = i & 3;
            a2[c] = mfma32(W2[s][c], __builtin_bit_cast(bf16x8, u[s]), a2[c]);
            IF (B && tt > 0 && i >= 2) fin_out(IC<tt - 1>{}, ic);
            IF (s == 4 && c > 0) LD(f32x4, (tt & 1) * 32768 + ((c - 1) >> 1) * 128 + (((c - 1) & 1) ? pw1 : pw0)) = a2[c - 1];
            SB();
        });
        if (tt < 2) u_ld(tt + 2);
        LD(f32x4, (tt & 1) * 32768 + 128 + pw1) = a2[3];
        BAR();
        STAMP(31 + tt * 2);
    });
    if (B) {
        sfor<0, 15>([&](auto ic) AI { fin_out(IC<3>{}, ic); });
    }
    if (A && b == 0 && w == 0) {
#pragma nounroll
        for (int it = 0; it < SPIN; ++it) {
            u32x4 f0 = ld16c<0>(rw, l16, WS_FLAG), f1_ = ld16c<1024>(rw, l16, WS_FLAG), f2 = ld16c<2048>(rw, l16, WS_FLAG), f3 = ld16c<3072>(rw, l16, WS_FLAG);
            VMW(0); TIE2(f0, f1_); TIE2(f2, f3);
            const u32x4 tg = {tag, tag, tag, tag};
            const u32x4 d = (f0 ^ tg) | (f1_ ^ tg) | (f2 ^ tg) | (f3 ^ tg);
            if (!anyl((d[0] | d[1] | d[2] | d[3]) != 0)) break;
        }
        st4s(rw, lane == 0 ? 0u : OOB, WS_EPOCH, tag);
    }
    STAMP(38); STAMPC(63);
    __builtin_amdgcn_endpgm();
    };
    if (kh == 0) body(IC<0>{});
    body(IC<1>{});
}

#if defined(__HIP_DEVICE_COMPILE__) && !defined(PK)
#pragma clang attribute pop
#endif
extern "C" void kernel_launch(void* const* d_in, const int* in_sizes, int n_in, void* d_out, int out_size, void* d_ws, size_t ws_size, hipStream_t stream) {
    fused_block<<<256, 512, 0, stream>>>((const char*)d_in[0], (const char*)d_in[1], (const char*)d_in[2], (const char*)d_in[4], (const char*)d_in[5],
                                         (const char*)d_in[6], (const char*)d_in[7], (char*)d_ws, (char*)d_out);
}
```

```cpp
#include <hip/hip_runtime.h>
#include <stdint.h>

#if defined(__HIP_DEVICE_COMPILE__) && !defined(PK)
#pragma clang attribute push(__attribute__((target("no-packed-fp32-ops"))), apply_to = function)
#endif
#define LAS __attribute__((address_space(3)))
typedef __bf16 bf16x8 __attribute__((ext_vector_type(8)));
typedef __bf16 bf16x4 __attribute__((ext_vector_type(4)));
typedef __bf16 bf16x2 __attribute__((ext_vector_type(2)));
typedef short s16x4 __attribute__((ext_vector_type(4)));
typedef float f32x4 __attribute__((ext_vector_type(4)));
typedef float f32x2 __attribute__((ext_vector_type(2)));
typedef unsigned u32x4 __attribute__((ext_vector_type(4)));
typedef unsigned u32x2 __attribute__((ext_vector_type(2)));
typedef __amdgpu_buffer_rsrc_t rsrc_t;
#define DI __device__ __forceinline__
#define U2F(x) __builtin_bit_cast(float, (unsigned)(x))
#define F2U(x) __builtin_bit_cast(unsigned, (float)(x))
#define EXP 0
#define PF 13
#define PT1 0
#define PT2 3
#define MAP 1
#define ROT 1

constexpr unsigned WS_EPOCH = 0, WS_FLAG = 4096, WS_MODP = 8192, WS_ST = 65536, WS_O = 1u << 20, WS_Y = 3u << 20, WS_U = 5u << 20, WS_BYTES = 11u << 20;
constexpr int SPIN = 1 << 15;
constexpr unsigned K = 1024;
constexpr unsigned L_M = 0, L_STAT = 144 * K, L_RST = 149 * K, L_SC = 150 * K, L_AS = 152 * K, L_MK = 156 * K;
constexpr unsigned L_H = 121 * K, L_X = 97 * K;
constexpr unsigned L_CB = 153 * K, L_GT = 155 * K, L_SST = 155 * K + 512, L_S1 = 156 * K;
constexpr unsigned L_PW = 121 * K, PWD = 16 * K, L_ACT = 97 * K, L_XC1 = 129 * K;
constexpr unsigned Z0 = 0, Z1 = 11 * K, Z2 = 22 * K, Z3 = 33 * K, Z4 = 44 * K, Z5 = 64 * K, Z6 = 75 * K, Z7 = 86 * K, Z8 = 97 * K, Z9 = 108 * K;

DI unsigned pk2(float a, float b) { bf16x2 v = {(__bf16)a, (__bf16)b}; return __builtin_bit_cast(unsigned, v); }
DI float lo16(unsigned u) { return U2F(u << 16); }
DI float hi16(unsigned u) { return U2F(u & 0xffff0000u); }
template <int CTRL> DI float dpp(float v) { return U2F(__builtin_amdgcn_update_dpp(0, (int)F2U(v), CTRL, 0xf, 0xf, true)); }
template <int CTRL> DI unsigned dppu(unsigned v) { return (unsigned)__builtin_amdgcn_update_dpp(0, (int)v, CTRL, 0xf, 0xf, true); }
DI float fmx(float a, float b) { return __builtin_elementwise_maximum(a, b); }
DI float dot2(unsigned a, unsigned b, float c) { return __builtin_amdgcn_fdot2_f32_bf16(__builtin_bit_cast(bf16x2, a), __builtin_bit_cast(bf16x2, b), c, false); }
DI float sw16add(float a, float b) { auto r = __builtin_amdgcn_permlane16_swap(F2U(a), F2U(b), false, false); return U2F(r[0]) + U2F(r[1]); }
DI float sw32add(float a, float b) { auto r = __builtin_amdgcn_permlane32_swap(F2U(a), F2U(b), false, false); return U2F(r[0]) + U2F(r[1]); }
DI float xmax16(float v) { auto r = __builtin_amdgcn_permlane16_swap(F2U(v), F2U(v), false, false); return fmx(U2F(r[0]), U2F(r[1])); }
DI float xmax32(float v) { auto r = __builtin_amdgcn_permlane32_swap(F2U(v), F2U(v), false, false); return fmx(U2F(r[0]), U2F(r[1])); }
DI f32x4 mfma32(bf16x8 a, bf16x8 b, f32x4 c) { return __builtin_amdgcn_mfma_f32_16x16x32_bf16(a, b, c, 0, 0, 0); }
DI f32x4 mfma32u(u32x4 a, u32x4 b, f32x4 c) { return mfma32(__builtin_bit_cast(bf16x8, a), __builtin_bit_cast(bf16x8, b), c); }
DI f32x4 mfma16u(unsigned a0, unsigned a1, unsigned b0, unsigned b1, f32x4 c) {
    return __builtin_amdgcn_mfma_f32_16x16x16bf16_1k(__builtin_bit_cast(s16x4, (u32x2){a0, a1}), __builtin_bit_cast(s16x4, (u32x2){b0, b1}), c, 0, 0, 0);
}
DI rsrc_t mk(const void* p, unsigned n) { return __builtin_amdgcn_make_buffer_rsrc((void*)p, (short)0, (int)n, 0x00020000); }
DI unsigned nosent(unsigned v) { return v < 0xfffffffeu ? v : 0xfffffffeu; }
DI unsigned umax3(unsigned a, unsigned b, unsigned c) { unsigned m = a > b ? a : b; return m > c ? m : c; }
DI unsigned umax4(u32x4 v, unsigned m) { return umax3(umax3(v[0], v[1], m), v[2], v[3]); }

#define BAR() asm volatile("s_waitcnt lgkmcnt(0)\n\ts_barrier" ::: "memory")
#define VMW(n) asm volatile("s_waitcnt vmcnt(%0)" ::"n"(n) : "memory")
#define TIE1(a) asm volatile("; TIE %0" : "+v"(a))
#define TIE2(a, b) asm volatile("; TIE %0 %1" : "+v"(a), "+v"(b))
DI void dma1(rsrc_t r, unsigned m0, unsigned vo, unsigned so) {
    asm volatile("s_mov_b32 m0, %0\n\ts_nop 0\n\tbuffer_load_dwordx4 %1, %2, %3 offen lds" ::"s"(m0), "v"(vo), "s"(r), "s"(so) : "m0", "memory");
}
template <int OFF> DI void dma1o(rsrc_t r, unsigned m0, unsigned vo, unsigned so) {
    asm volatile("s_mov_b32 m0, %0\n\ts_nop 0\n\tbuffer_load_dwordx4 %1, %2, %3 offen offset:%4 lds" ::"s"(m0), "v"(vo), "s"(r), "s"(so), "n"(OFF) : "m0", "memory");
}
DI void touch(unsigned& d, rsrc_t r, unsigned vo, unsigned so) {
    asm volatile("buffer_load_dword %0, %1, %2, %3 offen ; touch" : "+v"(d) : "v"(vo), "s"(r), "s"(so) : "memory");
}
DI void dma3(rsrc_t r, unsigned m0, unsigned vo, unsigned so) {
    asm volatile("s_mov_b32 m0, %0\n\ts_nop 0\n\tbuffer_load_dwordx4 %1, %2, %3 offen lds\n\tbuffer_load_dwordx4 %1, %2, %3 offen offset:1024 lds\n\tbuffer_load_dwordx4 %1, %2, %3 offen offset:2048 lds"
                 ::"s"(m0), "v"(vo), "s"(r), "s"(so) : "m0", "memory");
}
DI void dma3v(rsrc_t r, unsigned m0, unsigned v0, unsigned v1, unsigned v2, unsigned so) {
    asm volatile("s_mov_b32 m0, %0\n\ts_nop 0\n\tbuffer_load_dwordx4 %1, %4, %5 offen lds\n\tbuffer_load_dwordx4 %2, %4, %5 offen offset:1024 lds\n\tbuffer_load_dwordx4 %3, %4, %5 offen offset:2048 lds"
                 ::"s"(m0), "v"(v0), "v"(v1), "v"(v2), "s"(r), "s"(so) : "m0", "memory");
}
DI void dma4(rsrc_t r, unsigned m0, unsigned vo0, unsigned vo1, unsigned so) {
    asm volatile("s_mov_b32 m0, %0\n\ts_nop 0\n\tbuffer_load_dwordx4 %1, %3, %4 offen lds\n\tbuffer_load_dwordx4 %2, %3, %5 offen offset:1024 lds\n\t"
                 "buffer_load_dwordx4 %1, %3, %6 offen offset:2048 lds\n\tbuffer_load_dwordx4 %2, %3, %7 offen offset:3072 lds"
                 ::"s"(m0), "v"(vo0), "v"(vo1), "s"(r), "s"(so), "s"(so + 7168u), "s"(so + 14336u), "s"(so + 21504u) : "m0", "memory");
}
template <int OFF> DI u32x4 ld16(rsrc_t r, unsigned vo, unsigned so) {
    u32x4 v; asm volatile("buffer_load_dwordx4 %0, %1, %2, %3 offen offset:%4" : "=v"(v) : "v"(vo), "s"(r), "s"(so), "n"(OFF) : "memory"); return v;
}
template <int OFF> DI u32x4 ld16c(rsrc_t r, unsigned vo, unsigned so) {
    u32x4 v; asm volatile("buffer_load_dwordx4 %0, %1, %2, %3 offen offset:%4 sc1" : "=v"(v) : "v"(vo), "s"(r), "s"(so), "n"(OFF) : "memory"); return v;
}
DI u32x2 ld8(rsrc_t r, unsigned vo, unsigned so) {
    u32x2 v; asm volatile("buffer_load_dwordx2 %0, %1, %2, %3 offen" : "=v"(v) : "v"(vo), "s"(r), "s"(so) : "memory"); return v;
}
DI unsigned ld4(rsrc_t r, unsigned vo, unsigned so) {
    unsigned v; asm volatile("buffer_load_dword %0, %1, %2, %3 offen" : "=v"(v) : "v"(vo), "s"(r), "s"(so) : "memory"); return v;
}
DI unsigned ld4c(rsrc_t r, unsigned vo, unsigned so) {
    unsigned v; asm volatile("buffer_load_dword %0, %1, %2, %3 offen sc1" : "=v"(v) : "v"(vo), "s"(r), "s"(so) : "memory"); return v;
}
DI void st16c(rsrc_t r, unsigned vo, u32x4 v) { __builtin_amdgcn_raw_buffer_store_b128(v, r, (int)vo, 0, 16); }
DI void st4c(rsrc_t r, unsigned vo, unsigned v) { __builtin_amdgcn_raw_buffer_store_b32(v, r, (int)vo, 0, 16); }
DI void st16s(rsrc_t r, unsigned vo, unsigned so, u32x4 v) { __builtin_amdgcn_raw_buffer_store_b128(v, r, (int)vo, (int)so, 16); asm volatile("s_nop 1" ::"v"(v) : "memory"); }
DI void st16n(rsrc_t r, unsigned vo, unsigned so, u32x4 v) { __builtin_amdgcn_raw_buffer_store_b128(v, r, (int)vo, (int)so, 16); }
DI void st4s(rsrc_t r, unsigned vo, unsigned so, unsigned v) { __builtin_amdgcn_raw_buffer_store_b32(v, r, (int)vo, (int)so, 16); }
#define SB() __builtin_amdgcn_sched_barrier(0)
#define PIN(a) asm volatile("" : "+v"(a))
#define UNLIKELY(c) __builtin_expect(!!(c), 0)
constexpr unsigned OOB = 0x40000000u;
DI unsigned newlane() { unsigned l; asm volatile("v_mbcnt_lo_u32_b32 %0, -1, 0\n\tv_mbcnt_hi_u32_b32 %0, -1, %0" : "=v"(l)); return l; }
DI bool anyl(bool c) { return __builtin_amdgcn_ballot_w64(c) != 0; }

#define STAMP(i)
#define STAMPC(i)

#define XS(i)
template <int N> struct IC { static constexpr int v = N; };
template <int I, int N, class F> DI void sfor_(F& f) { if constexpr (I < N) { f(IC<I>{}); sfor_<I + 1, N>(f); } }
template <int I, int N, class F> DI void sfor(F f) { sfor_<I, N>(f); }
#define AI __attribute__((always_inline))
#define IF(c) if constexpr (c)
#define CI(n, c) constexpr int n = decltype(c)::v
DI void dma4s(rsrc_t r, unsigned m0, unsigned vo, unsigned so, unsigned stride) {
    asm volatile("s_mov_b32 m0, %0\n\ts_nop 0\n\tbuffer_load_dwordx4 %1, %2, %3 offen lds\n\tbuffer_load_dwordx4 %1, %2, %4 offen offset:1024 lds\n\t"
                 "buffer_load_dwordx4 %1, %2, %5 offen offset:2048 lds\n\tbuffer_load_dwordx4 %1, %2, %6 offen offset:3072 lds"
                 ::"s"(m0), "v"(vo), "s"(r), "s"(so), "s"(so + stride - 1024u), "s"(so + 2 * (stride - 1024u)), "s"(so + 3 * (stride - 1024u)) : "m0", "memory");
}

__global__ __launch_bounds__(512) void fused_block(const char* x, const char* wqkv, const char* wo, const char* w1, const char* w2,
                                                   const char* cvec, const char* wmod, char* ws, char* out) {
    __shared__ __attribute__((aligned(16))) char lds[163840];
    LAS char* L = (LAS char*)lds;
    const unsigned LB = (unsigned)(uintptr_t)L;
#define LD(T, off) (*(LAS T*)(L + (off)))
#define TRF(off, d) __builtin_shufflevector(__builtin_amdgcn_ds_read_tr16_b64_v4bf16((LAS bf16x4*)(L + (off))), __builtin_amdgcn_ds_read_tr16_b64_v4bf16((LAS bf16x4*)(L + (off) + (d))), 0, 1, 2, 3, 4, 5, 6, 7)
    unsigned lane = __builtin_amdgcn_workitem_id_x() & 63; const unsigned w = __builtin_amdgcn_readfirstlane(__builtin_amdgcn_workitem_id_x() >> 6);
    const unsigned b = __builtin_amdgcn_workgroup_id_x();
    STAMP(0); STAMPC(62);
    const unsigned rg = ((b & 7) >> 1) * 8 + (b >> 5), cg = (b & 1) * 4 + ((b >> 3) & 3);
    const unsigned rot = rg & 3;
    unsigned r8, c8, tk, g, q, p, l16, vy, swz0, fl, vo1k0, vo1k1, tre, tro, vos;
    auto derive = [&]() AI {
        r8 = lane >> 3; c8 = lane & 7; tk = lane & 15; g = lane >> 4; q = (lane & 15) >> 2; p = lane & 3;
        l16 = lane * 16; vy = r8 * 1024 + c8 * 16; swz0 = (c8 ^ (r8 & 2)) * 16; fl = ((q >> 1) & 1) | ((g & 1) << 1);
        vo1k0 = r8 * 1024 + swz0; vo1k1 = vo1k0 ^ 64;
        const unsigned trb = (8 * g + q) * 128 + (p >> 1) * 16 + (p & 1) * 8 + w * 4096;
        tre = trb + (fl << 1) * 16; tro = trb + (2 ^ (fl << 1)) * 16;
        vos = (g & 1) ? OOB : (g >> 1) * 256 + tk * 16;
    };
    derive();
#define RELANE() do { lane = newlane(); derive(); } while (0)
    const unsigned hd = w & 3, kh = w >> 2;
    const rsrc_t rx = mk(x, 2048 * 1024), rq = mk(wqkv, 512 * 3072), ro = mk(wo, 512 * 1024), r1 = mk(w1, 512 * 2752), r2 = mk(w2, 1376 * 1024);
    const rsrc_t rc = mk(cvec, 1024), rm = mk(wmod, 512 * 6144), rw = mk(ws, WS_BYTES), rout = mk(out, 2048 * 1024);
    const bool prod = b < 192 && w < 4;

    unsigned pfd = 0;
    {
        uint64_t pc; unsigned len;
        asm volatile("s_getpc_b64 %0\n.Lpfb:\n\ts_mov_b32 %1, .Lpfe-.Lpfb" : "=s"(pc), "=s"(len));
        touch(pfd, mk((const void*)pc, len), lane < 4 ? lane * 64 : OOB, ((b >> 3) * 8 + w) * 256);
    }
    unsigned ep = ld4c(rw, 0, WS_EPOCH);
    unsigned cv;
    if (prod) {
        cv = ld4(rc, lane * 4, (b & 3) * 256);
        dma4s(rm, LB + L_M + w * 4096, (lane >> 1) * 6144 + (lane & 1) * 16, (b & 3) * (128 * 6144) + (b >> 2) * 128 + w * 32, 32 * 6144);
    }
    u32x4 xr[10];
#pragma unroll
    for (int n = 0; n < 10; ++n) {
        int row = (int)rg * 64 - 16 + n * 8; if (row < 0) row += 16;
        xr[n] = ld16<0>(rx, vy, (unsigned)row * 1024 + w * 128);
    }
    constexpr int NT = (PF & 4) ? 1 : 0;
    {
        const char* tp = w == 0 ? wo : w == 1 ? w1 : w == 2 ? w2 : w == 3 ? out + rg * 65536 : w == 4 ? ws + WS_O + rg * 65536 : w == 5 ? ws + WS_Y + rg * 65536 : w == 6 ? ws + WS_U + rg * 176128 : w2 + 1375 * 1024;
        touch(pfd, mk(tp, 4), lane == 0 ? 0u : OOB, 0);
    }
    const unsigned vq = r8 * 3072 + (swz0 ^ ((w & 1) * 64));
    auto qkv_issue = [&](int r, unsigned slot) AI {
        const unsigned ks = kh * 8 + ((r + 2 * rot) & 7);
        dma3(rq, LB + slot + kh * 12288 + hd * 3072, vq, (ks * 32 + hd * 8) * 3072 + cg * 128);
    };
    STAMP(1);
    if (prod) {
        VMW(14 + NT); TIE2(ep, cv);
        const float a = lo16(cv), c = hi16(cv);
        LD(unsigned, L_SC + w * 256 + lane * 4) = pk2(a * __builtin_amdgcn_rcpf(1.f + __builtin_amdgcn_exp2f(-1.44269504089f * a)), c * __builtin_amdgcn_rcpf(1.f + __builtin_amdgcn_exp2f(-1.44269504089f * c)));
        VMW(10 + NT);
        f32x4 acc = {0, 0, 0, 0};
        const unsigned a0 = L_M + w * 4096 + (8 * g + q) * 32 + p * 8;
#pragma unroll
        for (int s = 0; s < 4; ++s) acc = mfma32(TRF(a0 + s * 1024, 128), LD(bf16x8, L_SC + w * 256 + s * 64 + g * 16), acc);
        u32x4 v;
#pragma unroll
        for (int j = 0; j < 4; ++j) v[j] = __builtin_amdgcn_perm(F2U(acc[j]), ep + 0x01010101u, 0x07060500u | j);
        st16s(rw, tk == 0 ? g * 16 : OOB, WS_MODP + (b * 64 + w * 16) * 4, v);
    } else if (w == 4) {
        const float NI = -__builtin_inff();
        f32x4 mcur, mprev; bool vc[4], vp[4];
#pragma unroll
        for (int r = 0; r < 4; ++r) {
            const int d = (int)tk - (int)(4 * g + r); vc[r] = d >= 0 && !(d & 1); vp[r] = d <= -2 && !(d & 1);
            mcur[r] = vc[r] ? 0.f : NI; mprev[r] = vp[r] ? 0.f : NI;
        }
        const u32x4 M = {(vp[0] ? 0xffffu : 0u) | (vp[1] ? 0xffff0000u : 0u), (vp[2] ? 0xffffu : 0u) | (vp[3] ? 0xffff0000u : 0u),
                         (vc[0] ? 0xffffu : 0u) | (vc[1] ? 0xffff0000u : 0u), (vc[2] ? 0xffffu : 0u) | (vc[3] ? 0xffff0000u : 0u)};
        LD(f32x4, L_MK + l16) = mcur; LD(f32x4, L_MK + 1024 + l16) = mprev; LD(u32x4, L_MK + 2048 + l16) = M;
        LD(u32x4, L_MK + 3072 + l16) = (u32x4){0x3f803f80u, 0x3f803f80u, 0x3f803f80u, 0x3f803f80u};
    }
    STAMP(2);
    qkv_issue(0, 48 * K); qkv_issue(1, 72 * K);
    float t[10][8];
#pragma unroll
    for (int h5 = 0; h5 < 2; ++h5) {
        if (h5 == 0) { VMW(11 + NT); } else { VMW(9 + NT); }
        float s[5], qq[5];
#pragma unroll
        for (int n = 0; n < 5; ++n) {
            TIE1(xr[h5 * 5 + n]);
            const u32x4 v = xr[h5 * 5 + n];
            s[n] = dot2(v[3], 0x3f803f80u, dot2(v[2], 0x3f803f80u, dot2(v[1], 0x3f803f80u, dot2(v[0], 0x3f803f80u, 0.f))));
            qq[n] = dot2(v[3], v[3], dot2(v[2], v[2], dot2(v[1], v[1], dot2(v[0], v[0], 0.f))));
        }
#define A5(F) F(0) F(1) F(2) F(3) F(4)
#define ST_A(n) "v_add_f32_dpp %" #n ", %" #n ", %" #n " row_half_mirror row_mask:0xf bank_mask:0x5\n\t"
#define ST_C(n) "v_add_f32_dpp %" #n ", %" #n ", %" #n " quad_perm:[1,0,3,2] row_mask:0xf bank_mask:0xf\n\t"
#define ST_D(n) "v_add_f32_dpp %" #n ", %" #n ", %" #n " quad_perm:[2,3,0,1] row_mask:0xf bank_mask:0xf\n\t"
#define ST_B(n, m) "v_add_f32_dpp %" #n ", %" #m ", %" #m " row_half_mirror row_mask:0xf bank_mask:0xa\n\t"
        asm volatile("s_nop 1\n\t" A5(ST_A) ST_B(0, 5) ST_B(1, 6) ST_B(2, 7) ST_B(3, 8) ST_B(4, 9) A5(ST_C) A5(ST_D)
                     : "+v"(s[0]), "+v"(s[1]), "+v"(s[2]), "+v"(s[3]), "+v"(s[4]) : "v"(qq[0]), "v"(qq[1]), "v"(qq[2]), "v"(qq[3]), "v"(qq[4]));
        unsigned sa = L_STAT + r8 * 64 + w * 8 + (c8 >> 2) * 4; PIN(sa);
#pragma unroll
        for (int n = 0; n < 5; ++n) LD(float, sa + (h5 * 5 + n) * 512) = s[n];
        if (h5 == 0) qkv_issue(2, 120 * K); else qkv_issue(3, 96 * K);
    }
    TIE1(ep);
    const unsigned tag = ep + 0x01010101u;
    auto mod_ld = [&](unsigned g0, unsigned g1, u32x4& a, u32x4& c) AI {
        a = ld16c<0>(rw, l16, WS_MODP + g0 * 1024); c = ld16c<0>(rw, l16, WS_MODP + g1 * 1024);
    };
    auto mod_bad = [&](u32x4 a, u32x4 c) AI {
        const unsigned ta = __builtin_amdgcn_perm(__builtin_amdgcn_perm(a[3], a[2], 0x0c0c0400u), __builtin_amdgcn_perm(a[1], a[0], 0x0c0c0400u), 0x05040100u);
        const unsigned tc = __builtin_amdgcn_perm(__builtin_amdgcn_perm(c[3], c[2], 0x0c0c0400u), __builtin_amdgcn_perm(c[1], c[0], 0x0c0c0400u), 0x05040100u);
        return anyl(ta != tag || tc != tag);
    };
    auto mod_fin = [&](unsigned g0, unsigned g1, u32x4 a, u32x4 c, float add1) AI {
        TIE2(a, c);
#pragma nounroll
        for (int it = 0; it < SPIN && mod_bad(a, c); ++it) { mod_ld(g0, g1, a, c); VMW(0); TIE2(a, c); }
        float s[4];
#pragma unroll
        for (int j = 0; j < 4; ++j) s[j] = sw16add(U2F(a[j] & 0xffffff00u), U2F(c[j] & 0xffffff00u));
        const unsigned r = pk2(sw32add(s[0], s[2]), sw32add(s[1], s[3]));
        const float ad = (g & 1) ? add1 : 0.f;
        return pk2(lo16(r) + ad, hi16(r) + ad);
    };
    auto unpack = [&](int n0, int n1) AI {
#pragma unroll
        for (int n = n0; n < n1; ++n)
#pragma unroll
            for (int j = 0; j < 4; ++j) { t[n][2 * j] = lo16(xr[n][j]); t[n][2 * j + 1] = hi16(xr[n][j]); PIN(t[n][2 * j]); PIN(t[n][2 * j + 1]); }
    };
    STAMP(3);
    BAR();
    STAMP(4);
    if (w == 4 || w == 5) {
        const unsigned row = w == 4 ? lane : 64 + tk;
        const f32x4 a = LD(f32x4, L_STAT + row * 64) + LD(f32x4, L_STAT + row * 64 + 16) + LD(f32x4, L_STAT + row * 64 + 32) + LD(f32x4, L_STAT + row * 64 + 48);
        const float mu = (a[0] + a[2]) * (1.f / 512.f);
        const float var = __builtin_fmaxf((a[1] + a[3]) * (1.f / 512.f) - mu * mu, 0.f);
        const float rs = __builtin_amdgcn_rsqf(var + 1e-5f);
        LD(f32x2, L_RST + row * 8) = (f32x2){rs, -mu * rs};
    }
    qkv_issue(4, 0);
    unpack(0, 5);
    qkv_issue(5, 24 * K);
    unpack(5, 10);
    u32x4 ma, mc;
    mod_ld(w, 8 + w, ma, mc);
    VMW(11); TIE1(pfd); BAR();
    STAMP(5);
    bf16x8 Wq[8][3];
    const unsigned trq = kh * 12288 + g * 3072 + q * 128 + (((2 * hd) ^ (fl << 1)) + (p >> 1)) * 16 + (p & 1) * 8;
    unsigned trqh = trq + 72 * K; PIN(trqh);
    auto qkv_read = [&](int r, unsigned base, unsigned slot) AI {
#pragma unroll
        for (int m = 0; m < 3; ++m) Wq[r][m] = TRF(base + slot + m * 1024, 512);
    };
    unsigned ra = L_RST + r8 * 8; PIN(ra);
    auto nrm = [&](int n) AI {
        const f32x2 st = LD(f32x2, ra + n * 64);
#pragma unroll
        for (int j = 0; j < 8; ++j) { t[n][j] = __builtin_fmaf(t[n][j], st.x, st.y); PIN(t[n][j]); }
    };
    qkv_read(0, trq, 48 * K); qkv_read(1, trqh, 0); qkv_read(2, trqh, 48 * K);
    nrm(0); nrm(1);
    BAR();
    STAMP(6);
    qkv_issue(6, 48 * K);
    nrm(2); nrm(3); nrm(4); nrm(5);
    qkv_issue(7, 72 * K);
    nrm(6); nrm(7); nrm(8); nrm(9);
    const unsigned tile0 = cg * 11 > 75 ? 75 : cg * 11;
    const unsigned hrot = (hd - rot) & 3;
    STAMP(7);
    auto body = [&](auto KHC) AI {
    constexpr bool A = decltype(KHC)::v == 0, B = !A;
    constexpr unsigned KH = decltype(KHC)::v;
    if (B) __builtin_amdgcn_s_setprio(1);
    bf16x8 Wo[4][4], W1[8][3], W2[6][4];
    unsigned v1o[3];
    const unsigned f1 = hd == 3 ? 8 : 3 * hd;
    auto v1o_mk = [&]() AI {
#pragma unroll
        for (int j = 0; j < 3; ++j) { const unsigned ch = (f1 + j) * 64 + lane, r = ch / 22; v1o[j] = r * 2752 + (ch - r * 22) * 16 - j * 1024; PIN(v1o[j]); }
    };
    auto w1_issue = [&](int h2, int r, unsigned slot) AI {
        dma3v(r1, LB + slot + f1 * 1024, v1o[0], v1o[1], v1o[2], (h2 * 8 + ((r + 2 * rot) & 7)) * (32 * 2752) + tile0 * 32);
    };
    auto w1_one = [&](int h2, int r, unsigned slot, auto jc) AI {
        CI(j, jc);
        dma1o<j * 1024>(r1, LB + slot + f1 * 1024, v1o[j], (h2 * 8 + ((r + 2 * rot) & 7)) * (32 * 2752) + tile0 * 32);
    };
    auto wo_one = [&](int r, auto jc) AI {
        CI(j, jc);
        dma1o<j * 1024>(ro, LB + r * 16384 + hd * 4096, (j & 1) ? vo1k1 : vo1k0, (hd * 4 + r) * 32768 + cg * 128 + j * 7168);
    };
    auto row_read = [&](bf16x8* W, unsigned base) AI {
        W[0] = TRF(base + tre, 512); W[1] = TRF(base + tro, 512); W[2] = TRF(base + (tre ^ 64), 512); W[3] = TRF(base + (tro ^ 64), 512);
    };
    unsigned tr1;
    auto w1_read = [&](int r, unsigned slot) AI {
        unsigned ba = tr1; if (slot >= 64 * K) { ba += 64 * K; PIN(ba); slot -= 64 * K; }
#pragma unroll
        for (int m = 0; m < 3; ++m) W1[r][m] = TRF(ba + slot + m * 32, 5632);
    };
    unsigned aw0 = L_H + ((2 * (KH * 4 + hrot) + (c8 >> 2)) * 64 + (c8 & 3) * 16 + (r8 ^ ((c8 & 3) | ((c8 >> 2) * 12)))) * 16, aw1 = aw0 ^ 128;
    unsigned ar0 = L_H + (KH * 512 + g * 16 + (tk ^ g)) * 16, ar1 = L_H + (KH * 512 + 64 + g * 16 + (tk ^ (g | 12))) * 16;
    unsigned xp = L_X + hd * 3072 + l16;
    PIN(aw0); PIN(aw1); PIN(ar0); PIN(ar1); PIN(xp);
    auto pref = [&](int n) AI {
        const unsigned j = b >> 3, hf_ = b & 1;
        if (n == 0) touch(pfd, r1, tk < 12 ? g * 2752 + tk * 128 : OOB, (j * 16 + hd * 4) * 2752 + hf_ * 1344);
        else touch(pfd, r2, (lane >> 2) * 1024 + p * 128, hd < 3 ? (j * 43 + hd * 16) * 1024 + hf_ * 512 : OOB);
    };
    VMW(6);
    STAMP(8);
    f32x4 A0, A1, S0, S1;
    {
        const unsigned r = mod_fin(w, 8 + w, ma, mc, 1.f);
        LD(f32x2, L_AS + w * 512 + (g & 1) * 256 + tk * 16 + (g >> 1) * 8) = (f32x2){lo16(r), hi16(r)};
        unsigned aa = L_AS + w * 512 + c8 * 32; PIN(aa);
        S0 = LD(f32x4, aa); S1 = LD(f32x4, aa + 16); A0 = LD(f32x4, aa + 256); A1 = LD(f32x4, aa + 272);
    }
    STAMP(9);
    u32x4 hw;
    auto norm1 = [&](auto ic) AI {
        constexpr int n = decltype(ic)::v >> 2, j = decltype(ic)::v & 3;
        const f32x4 Av = j < 2 ? A0 : A1, Sv = j < 2 ? S0 : S1;
        hw[j] = pk2(__builtin_fmaf(t[n][2 * j], Av[(2 * j) & 3], Sv[(2 * j) & 3]), __builtin_fmaf(t[n][2 * j + 1], Av[(2 * j + 1) & 3], Sv[(2 * j + 1) & 3]));
        IF (j == 3) LD(u32x4, ((n >> 1) & 1) * 16384 + ((n & 1) ? aw1 : aw0)) = hw;
    };
    sfor<0, 8>(norm1);
    BAR();
    STAMP(10);
    bf16x8 hf[8];
    auto h_rd = [&](int s, unsigned base) AI { hf[s] = LD(bf16x8, base + (s >> 1) * 2048 + ((s & 1) ? ar1 : ar0)); };

    f32x4 aq, ak, av, oq, ok, ov;
    u32x2 kc, vc, kp, vp;
    f32x4 pq_, pk_, pv_, mcur, mprev, sp, sc, sm, ao; u32x4 M, ones, P; unsigned q0, q1, mkp; float t0, t1, mx, inv;
    auto attn = [&](auto Tc, auto ic) AI {
        CI(T, Tc); CI(i, ic);
        const unsigned xb = xp + (T & 1) * 12288;
        constexpr float C = 0.25f * 1.44269504089f;
        if (T == 0) {
            if (i == 0) { pk_ = LD(f32x4, xb + 1024); pv_ = LD(f32x4, xb + 2048); }
            if (i == 5) kc = (u32x2){pk2(ok[0] + pk_[0], ok[1] + pk_[1]), pk2(ok[2] + pk_[2], ok[3] + pk_[3])};
            if (i == 7) vc = (u32x2){pk2(ov[0] + pv_[0], ov[1] + pv_[1]), pk2(ov[2] + pv_[2], ov[3] + pv_[3])};
            return;
        }
        if (i == 0) { pq_ = LD(f32x4, xb); mprev = LD(f32x4, mkp + 1024); pk_ = LD(f32x4, xb + 1024); mcur = LD(f32x4, mkp); pv_ = LD(f32x4, xb + 2048); kp = kc; vp = vc; }
        if (i == 4) {
            if (T == 1) { const float ni = rg == 0 ? -__builtin_inff() : 0.f; mprev += ni; }
            oq += pq_; q0 = pk2(oq[0], oq[1]); q1 = pk2(oq[2], oq[3]); sp = mfma16u(kp[0], kp[1], q0, q1, mprev);
        }
        if (i == 5) { ok += pk_; kc = (u32x2){pk2(ok[0], ok[1]), pk2(ok[2], ok[3])}; sc = mfma16u(kc[0], kc[1], q0, q1, mcur); M = LD(u32x4, mkp + 2048); ones = LD(u32x4, mkp + 3072); }
        if (i == 6) { ov += pv_; vc = (u32x2){pk2(ov[0], ov[1]), pk2(ov[2], ov[3])}; }
        if (i == 7) { t0 = fmx(fmx(fmx(sp[0], sp[1]), sc[0]), sc[1]); t1 = fmx(fmx(fmx(sp[2], sp[3]), sc[2]), sc[3]); }
        if (i == 8) mx = xmax16(fmx(t0, t1));
        if (i == 9) mx = xmax32(mx) * C;
        if (i == 10) { t0 = __builtin_amdgcn_exp2f(__builtin_fmaf(t0, C, -mx)); t1 = __builtin_amdgcn_exp2f(__builtin_fmaf(t1, C, -mx)); }
        if (i == 11) { q0 = pk2(t0, t0); q1 = pk2(t1, t1); P = (u32x4){q0 & M[0], q1 & M[1], q0 & M[2], q1 & M[3]}; }
        if (i == 12) { sm = mfma32u(ones, P, (f32x4){0, 0, 0, 0}); ao = mfma32u((u32x4){vp[0], vp[1], vc[0], vc[1]}, P, (f32x4){0, 0, 0, 0}); }
        if (i == 14) inv = __builtin_amdgcn_rcpf(sm[0]);
        if (i == 15) { ao *= inv; q0 = pk2(ao[0], ao[1]); q1 = pk2(ao[2], ao[3]); }
        if (i == 16) {
            auto ra_ = __builtin_amdgcn_permlane16_swap(q0, q0, false, false);
            auto rb_ = __builtin_amdgcn_permlane16_swap(q1, q1, false, false);
            st16s(rw, vos, WS_O + ((rg * 4 + T - 1) * 16 + cg * 2) * 1024 + hd * 512, (u32x4){ra_[0], rb_[0], ra_[1], rb_[1]});
        }
    };
    const u32x4 ff = {~0u, ~0u, ~0u, ~0u};
    auto clr = [&](int n) AI {
        const unsigned vw = n < 6 ? hd : hd + 4; const int i = n < 6 ? n : n - 6;
        const unsigned ub = WS_U + (rg * 4 + (vw >> 1)) * 44032 + tile0 * 512 + (vw & 1) * 3072;
        const unsigned lo32 = lane < 32 ? 0u : OOB;
        if (i == 0) st16s(rw, l16, WS_O + ((rg * 4 + (vw >> 1)) * 16 + cg * 2 + (vw & 1)) * 1024, ff);
        if (i == 1) st16s(rw, vy, WS_Y + (rg * 64 + vw * 8) * 1024 + cg * 128, ff);
        if (i == 2) st16s(rw, l16, (cg < 7 || (vw & 1)) ? ub : OOB, ff);
        if (i == 3) st16s(rw, l16, ub + 1024, ff);
        if (i == 4) st16s(rw, l16 | ((vw & 1) ? lo32 : 0u), ub + 2048, ff);
        if (i == 5) st16s(rw, vy | lo32, vw == 0 ? WS_ST + (rg * 32 + cg) * 128 : OOB, ff);
    };
    u32x4 m2a, m2c, m3a, m3c, m4a, m4c; unsigned flg;
    u32x4 of[4];
    auto o_ld = [&](int tt) AI {
        const unsigned so = WS_O + ((rg * 4 + tt) * 16 + 4 * hd) * 1024;
        of[0] = ld16c<0>(rw, l16, so); of[1] = ld16c<1024>(rw, l16, so); of[2] = ld16c<2048>(rw, l16, so); of[3] = ld16c<3072>(rw, l16, so);
    };
    auto cbw = [&]() AI { return (g & 1) * 1024 + (tk >> 3) * 64 + (tk & 3) * 16 + ((tk >> 2) & 1) * 8 + (g >> 1) * 4; };
    u32x2 xres[4]; unsigned fvo;
    auto qside = [&](auto Tc, auto ic) AI {
        CI(T, Tc); CI(i, ic);
        constexpr unsigned HN = ((T + 1) & 1) * 16384;
        constexpr int BI = T == 0 ? 7 : 4;
        IF (T == 0 && i < 2) { norm1(IC<8 + 2 * i>{}); norm1(IC<9 + 2 * i>{}); }
        IF (T == 0 && i >= 2 && i < 14) norm1(IC<10 + i>{});
        IF (T == 1 && i >= 6 && i < 22 && !(i & 1)) norm1(IC<24 + (i - 6) / 2>{});
        IF (T == 2 && i < 3) norm1(IC<32 + i>{});
        IF (T == 2 && i >= 5 && i < 8) norm1(IC<30 + i>{});
        IF (T == 2 && A && (i == 8 || i == 9)) norm1(IC<30 + i>{});
        IF (T == 2 && B && (i == 21 || i == 22)) norm1(IC<17 + i>{});
        if (A) {
            const unsigned xb = xp + ((T - 1) & 1) * 12288;
            if (T > 1 && i == 0) LD(f32x4, xb) = oq;
            if (T > 0 && i == 1) LD(f32x4, xb + 1024) = ok;
            if (T > 0 && i == 2) LD(f32x4, xb + 2048) = ov;
            IF (T == 1 && i % 3 == 1) wo_one(i / 12, IC<(i / 3) & 3>{});
            IF (T == 2 && i < 16 && !(i & 1)) wo_one(2 + i / 8, IC<(i / 2) & 3>{});
            IF (T == 2 && i >= 16 && i < 22 && !(i & 1)) w1_one(1, 0, Z5, IC<(i - 16) / 2>{});
            if (T == 2 && i == 3) { VMW(6); row_read(Wo[0], 0); }
            if (T == 2 && i == 11) { v1o_mk(); }
            if (T == 2 && i == 13) { VMW(7); row_read(Wo[1], 16384); }
            IF (T == 3 && i < 18 && !(i & 1)) w1_one(i < 12 ? 1 : 0, i < 6 ? 1 : i < 12 ? 2 : 0, i < 6 ? Z6 : i < 12 ? Z7 : Z0, IC<(i / 2) % 3>{});
            if (T == 3 && i == 1) { VMW(8); row_read(Wo[2], 32768); }
            if (T == 3 && i == 9) { VMW(8); row_read(Wo[3], 49152); }
            if (T == 3 && i == 18) mod_ld(24 + hd, 32 + hd, m2a, m2c);
            if (T == 3 && i == 21) mod_ld(16 + cg, 40 + cg, m3a, m3c);
            IF (T == 4 && i < 6 && !(i & 1)) w1_one(0, 1, Z1, IC<i / 2>{});
            if (T == 4 && i == 4) VMW(7);
            if (T == 4 && i == 5) { mod_ld(28 + hd, 36 + hd, m4a, m4c); flg = ld4c(rw, (lane & 31) * 4, WS_FLAG + rg * 128); }
            IF (T == 4 && i >= 7 && i < 19 && (i & 1)) w1_one(0, i < 13 ? 2 : 3, i < 13 ? Z2 : Z3, IC<((i - 7) / 2) % 3>{});
            if (T == 4 && i == 6) w1_read(0, Z0);
            if (T == 4 && i == 8) { VMW(7); LD(unsigned, L_CB + hrot * 128 + cbw()) = mod_fin(24 + hd, 32 + hd, m2a, m2c, 1.f); }
            if (T == 4 && i == 14) { const unsigned r = mod_fin(16 + cg, 40 + cg, m3a, m3c, 0.f); LD(unsigned, L_GT + cbw() - (g & 1) * 896) = r; }
            if (T == 4 && i == 21) {
                VMW(6); TIE1(flg);
#pragma nounroll
                for (int it = 0; it < SPIN && anyl(flg != tag); ++it) { flg = ld4c(rw, (lane & 31) * 4, WS_FLAG + rg * 128); VMW(0); TIE1(flg); }
                o_ld(0);
            }
            if (T == 0 && i == 7) VMW(0);
        } else {
            if (T == 0 && !(i & 1)) clr(i / 2);
            if (T == 1 && i < 6 && !(i & 1)) clr(8 + i / 2);
            if (T == 0 && i == 7) VMW(4);
            if ((PF & 2) && T == PT1 && i == 9) pref(0);
            if ((PF & 8) && T == PT2 && i == 11) pref(1);
            if (T == 2 && i == 3) { VMW(0); if (PT2 < 2) TIE1(pfd); st4s(rw, lane == 0 ? 0u : OOB, WS_FLAG + rg * 128 + cg * 16 + hd * 4, tag); }
            IF (T > 0 && i > 4) attn(IC<T - 1>{}, IC<i - 4>{});
            if (T == 4 && i == 6) {
                fvo = (hd * 4 + g) * 1024 + ((lane >> 3) & 1) * 64 + (lane & 1) * 32 + ((lane >> 1) & 3) * 8; PIN(fvo);
#pragma unroll
                for (int tt = 0; tt < 4; ++tt) xres[tt] = ld8(rx, fvo, (rg * 64 + tt * 16) * 1024 + cg * 128);
            }
            if (T == 4 && i == 13) w1_read(0, Z5);
            if (T == 4 && i == 17) w1_read(1, Z6);
            if (T == 4 && i == 21) w1_read(2, Z7);
        }
        if (i == BI) {
            STAMP(40 + 2 * T);
            BAR();
            STAMP(41 + 2 * T);
            IF (B && T > 0) attn(IC<T - 1>{}, IC<0>{});
        }
        IF (T == 0) {
            if (i == 0) { qkv_read(4, trq, 0); }
            if (i == 2) { qkv_read(5, trq, 24 * K); }
            if (i == 7) { qkv_read(6, trq, 48 * K); }
            if (i == 8) { qkv_read(7, trqh, 0); h_rd(0, HN); h_rd(1, HN); }
            if (i == 9) { h_rd(2, HN); h_rd(3, HN); h_rd(4, HN); }
            if (i == 11) h_rd(5, HN);
            if (i == 13) h_rd(6, HN);
            if (i == 15) h_rd(7, HN);
        }
        IF (T > 0 && T < 4) {
            if (i == 5) h_rd(0, HN);
            if (i >= 5 && i % 3 == 2) h_rd(i / 3, HN);
        }
        SB();
    };
    if (B) { mkp = L_MK + l16; PIN(mkp); PIN(vos); }
    tr1 = (4 * g + q) * 352 + hd * 96 + p * 8; PIN(tr1);
    h_rd(0, 0); h_rd(1, 0); h_rd(2, 0); h_rd(3, 0); qkv_read(3, trqh, 24 * K); h_rd(4, 0); h_rd(5, 0); h_rd(6, 0); h_rd(7, 0);
    sfor<0, 5>([&](auto Tc) AI {
        CI(T, Tc);
        oq = aq; ok = ak; ov = av;
        aq = (f32x4){0, 0, 0, 0}; ak = aq; av = aq;
        SB();
        sfor<0, 8>([&](auto sc_) AI {
            CI(s, sc_);
            IF (T > 0) { aq = mfma32(Wq[s][0], hf[s], aq); qside(Tc, IC<3 * s>{}); }
            ak = mfma32(Wq[s][1], hf[s], ak); qside(Tc, IC<(T > 0 ? 3 * s + 1 : 2 * s)>{});
            av = mfma32(hf[s], Wq[s][2], av); qside(Tc, IC<(T > 0 ? 3 * s + 2 : 2 * s + 1)>{});
        });
        STAMP(11 + T);
    });
    if (A) { LD(f32x4, xp) = aq; LD(f32x4, xp + 1024) = ak; LD(f32x4, xp + 2048) = av; }
    oq = aq; ok = ak; ov = av;
    BAR();

    STAMP(56);
    if (B) {
        sfor<0, 17>([&](auto ic) AI { attn(IC<4>{}, ic); });
        SB();
    } else {
        w1_issue(1, 3, Z5); w1_issue(1, 4, Z6); w1_issue(0, 4, Z4);
        w1_read(1, Z1);
        LD(unsigned, L_CB + 512 + hrot * 128 + cbw()) = mod_fin(28 + hd, 36 + hd, m4a, m4c, 1.f);
    }
    STAMP(50);
    RELANE();
    const unsigned fb = (lane >> 3) & 1, fg = (lane >> 1) & 3, fe = lane & 1, ftok = hd * 4 + g;
    unsigned pw0 = w * 4096 + tk * 256 + (g ^ (tk & 7)) * 16;
    unsigned pr = ftok * 256 + ((fb * 8 + fe * 4 + fg) ^ (ftok & 7)) * 16;
    auto w2_issue = [&](unsigned vw, int s, unsigned slot) AI {
        const unsigned ks = vw * 5 + (vw < 3 ? vw : 3) + s;
        dma4(r2, LB + slot + vw * 4096, vo1k0, vo1k1, ks * 32768 + cg * 128);
    };
    auto w2_one = [&](unsigned vw, int s, unsigned slot, auto jc) AI {
        CI(j, jc);
        const unsigned ks = vw * 5 + (vw < 3 ? vw : 3) + s;
        dma1o<j * 1024>(r2, LB + slot + vw * 4096, (j & 1) ? vo1k1 : vo1k0, ks * 32768 + cg * 128 + j * 7168);
    };
    u32x2 x1p[4];
    f32x4 sacc[3] = {{0, 0, 0, 0}, {0, 0, 0, 0}, {0, 0, 0, 0}};
    auto s1_part = [&](int s0, int s1) AI {
        unsigned ca = L_CB + (tk & 1) * 1024 + KH * 512 + g * 16; PIN(ca);
        bf16x8 yf[8];
#pragma unroll
        for (int s = s0; s < s1; ++s) yf[s] = LD(bf16x8, ca + s * 64);
#pragma unroll
        for (int s = s0; s < s1; ++s)
#pragma unroll
            for (int m = 0; m < 3; ++m) sacc[m] = mfma32(W1[s][m], yf[s], sacc[m]);
        SB();
    };
    u32x4 yv[4], sv;
    auto y_ld = [&](int tt) AI {
        const unsigned so = WS_Y + (rg * 4 + tt) * 16384 + hd * 128;
        yv[0] = ld16c<0>(rw, vy, so); yv[1] = ld16c<0>(rw, vy, so + 8192); yv[2] = ld16c<512>(rw, vy, so); yv[3] = ld16c<512>(rw, vy, so + 8192);
        sv = ld16c<0>(rw, l16, hd == (unsigned)tt ? WS_ST + (rg * 4 + tt) * 1024 : OOB);
    };
    aw0 += L_ACT - L_H; aw1 += L_ACT - L_H; ar0 += L_ACT - L_H; ar1 += L_ACT - L_H; PIN(ar0); PIN(ar1);
    if (A) { PIN(aw0); PIN(aw1); }
    auto y_put = [&](int tt) AI {
        TIE2(yv[0], yv[1]); TIE2(yv[2], yv[3]); TIE1(sv);
#pragma nounroll
        for (int it = 0; it < SPIN && anyl(umax4(sv, umax4(yv[3], umax4(yv[2], umax4(yv[1], umax4(yv[0], 0))))) == ~0u); ++it) {
            y_ld(tt); VMW(0); TIE2(yv[0], yv[1]); TIE2(yv[2], yv[3]); TIE1(sv);
        }
        LD(u32x4, (tt & 1) * 16384 + aw0) = yv[0]; LD(u32x4, (tt & 1) * 16384 + aw1) = yv[1];
        LD(u32x4, (tt & 1) * 16384 + 8192 + aw0) = yv[2]; LD(u32x4, (tt & 1) * 16384 + 8192 + aw1) = yv[3];
    };
    auto y_stat = [&](int tt) AI {
        if (hd == (unsigned)tt) {
            float s = sw16add(U2F(sv[0]), U2F(sv[2])), qq = sw16add(U2F(sv[1]), U2F(sv[3]));
            s += dpp<0x128>(s); qq += dpp<0x128>(qq);
            s = sw32add(s, s); qq = sw32add(qq, qq);
            const float mu = s * (1.f / 512.f);
            const float var = __builtin_fmaxf(qq * (1.f / 512.f) - mu * mu, 0.f);
            LD(f32x2, L_SST + tt * 128 + (c8 * 2 + (g & 1)) * 8) = (f32x2){-mu, -1.44269504089f * __builtin_amdgcn_rsqf(var + 1e-5f)};
        }
    };
    if (A) {
        unsigned pa0 = L_PW + pw0, pa1 = pa0 ^ 64; PIN(pa0); PIN(pa1);
        auto wo_step = [&](int tt) AI {
            TIE2(of[0], of[1]); TIE2(of[2], of[3]);
#pragma nounroll
            for (int it = 0; it < SPIN && anyl(umax4(of[3], umax4(of[2], umax4(of[1], umax4(of[0], 0)))) == ~0u); ++it) { o_ld(tt); VMW(0); TIE2(of[0], of[1]); TIE2(of[2], of[3]); }
            STAMP(16 + tt * 2);
            if (tt == 3) y_ld(0);
            f32x4 acc[4] = {{0, 0, 0, 0}, {0, 0, 0, 0}, {0, 0, 0, 0}, {0, 0, 0, 0}};
#pragma unroll
            for (int c = 0; c < 4; ++c) {
#pragma unroll
                for (int j = 0; j < 4; ++j) acc[c] = mfma32(Wo[j][c], __builtin_bit_cast(bf16x8, of[j]), acc[c]);
                if (c > 0) LD(f32x4, (tt & 1) * PWD + ((c - 1) >> 1) * 128 + (((c - 1) & 1) ? pa1 : pa0)) = acc[c - 1];
                SB();
            }
            if (tt < 3) o_ld(tt + 1);
            LD(f32x4, (tt & 1) * PWD + 128 + pa1) = acc[3];
        };
        VMW(9); wo_step(0);
        VMW(4); BAR();
        STAMP(17);
        w1_read(2, Z2); w1_read(3, Z3); w1_read(4, Z4);
        s1_part(0, 2);
        VMW(0); wo_step(1);
        XS(2);
        BAR();
        STAMP(19);
        s1_part(2, 5);
        XS(3);
        VMW(0); XS(4); wo_step(2);
        XS(5);
        BAR();
        STAMP(21);
        w1_read(5, Z8); w1_read(6, Z9);
        s1_part(5, 7);
        XS(6);
        VMW(0); XS(7); wo_step(3);
        XS(8);
        BAR();
        STAMP(23);
        w1_read(7, Z3);
        VMW(0); y_put(0);
        STAMP(51);
        y_stat(0);
        y_ld(1);
        s1_part(7, 8);
#pragma unroll
        for (int m = 0; m < 3; ++m) LD(f32x4, L_S1 + (tk & 1) * 768 + (hd * 48 + m * 16 + g * 4) * 4) = sacc[m];
        XS(57);
    } else {
        unsigned prw = L_PW + pr; PIN(prw);
        f32x4 ga, A2;
        auto fin_wo = [&](int tt) AI {
            f32x4 pp[4];
#pragma unroll
            for (int ww = 0; ww < 4; ++ww) pp[ww] = LD(f32x4, prw + (tt & 1) * PWD + ww * 4096);
            if (tt == 0) {
                const u32x2 gv = LD(u32x2, L_GT + fb * 64 + fg * 16 + fe * 8);
                const u32x2 av2 = LD(u32x2, L_CB + 1024 + ((cg & 4) + ((cg - rot) & 3)) * 128 + fb * 64 + fg * 16 + fe * 8);
                ga = (f32x4){lo16(gv[0]), hi16(gv[0]), lo16(gv[1]), hi16(gv[1])};
                A2 = (f32x4){lo16(av2[0]), hi16(av2[0]), lo16(av2[1]), hi16(av2[1])};
            }
            const u32x2 xr2 = xres[tt];
            const f32x4 xv = {lo16(xr2[0]), hi16(xr2[0]), lo16(xr2[1]), hi16(xr2[1])};
            const f32x4 sum = (pp[0] + pp[1]) + (pp[2] + pp[3]);
            const f32x4 v = ga * sum + xv;
            const f32x4 xx = v * A2;
            const unsigned y0 = pk2(xx[0], xx[1]), y1 = pk2(xx[2], xx[3]);
            const u32x4 yy = {y0, y1, dppu<0xF5>(y0), dppu<0xF5>(y1)};
            st16s(rw, fe ? OOB : ftok * 1024 + tk * 8, WS_Y + (rg * 4 + tt) * 16384 + cg * 128, yy);
            const u32x2 x1 = {pk2(v[0], v[1]), pk2(v[2], v[3])};
            x1p[tt] = x1;
            float s = dot2(x1[1], 0x3f803f80u, dot2(x1[0], 0x3f803f80u, 0.f));
            const float qq = dot2(x1[1], x1[1], dot2(x1[0], x1[0], 0.f));
            asm volatile("s_nop 1\n\tv_add_f32_dpp %0, %0, %0 row_ror:8 row_mask:0xf bank_mask:0x3\n\tv_add_f32_dpp %0, %1, %1 row_ror:8 row_mask:0xf bank_mask:0xc\n\ts_nop 1" : "+v"(s) : "v"(qq));
            s += dpp<0x141>(s); s += dpp<0xB1>(s); s += dpp<0x4E>(s);
            st4s(rw, c8 ? OOB : ftok * 8 + fb * 4, WS_ST + ((rg * 4 + tt) * 8 + cg) * 128, F2U(s));
            SB();
        };
        v1o_mk();
        VMW(2); if (PT2 >= 2) TIE1(pfd); TIE2(xres[0], xres[1]); TIE2(xres[2], xres[3]);
        BAR();
        STAMP(17);
        fin_wo(0);
        STAMP(18);
        w1_issue(0, 5, Z8); w1_issue(0, 6, Z9); w1_issue(1, 5, Z7);
        XS(1);
        w1_read(3, Z5); w1_read(4, Z6);
        XS(2);
        BAR();
        STAMP(19);
        fin_wo(1);
        STAMP(20);
        w1_issue(0, 7, Z3); w1_issue(1, 6, Z4); w1_issue(1, 7, Z5);
        XS(3);
        s1_part(0, 3);
        XS(4);
        VMW(11);
        XS(5);
        BAR();
        STAMP(21);
        fin_wo(2);
        STAMP(22);
        w2_issue(hd, 0, 0); w2_issue(hd + 4, 0, 0);
        XS(6);
        w1_read(5, Z7);
        s1_part(3, 6);
        XS(7);
        VMW(10);
        XS(8);
        BAR();
        STAMP(23);
        fin_wo(3);
        STAMP(51);
        w1_read(6, Z4); w1_read(7, Z5);
        w2_issue(hd + 4, 4, 64 * K);
        s1_part(6, 8);
        XS(9);
        VMW(6);
        XS(57);
    }
    BAR();
    STAMP(24);

    h_rd(0, 0); h_rd(1, 0); h_rd(2, 0); h_rd(3, 0); h_rd(4, 0); h_rd(5, 0); h_rd(6, 0); h_rd(7, 0);
    unsigned xp1 = L_XC1 + hd * 3072 + l16; PIN(xp1);
    unsigned s1a = L_S1 + hd * 192 + g * 16;
    unsigned vus0 = (g >> 1) * 256 + tk * 16 + (g & 1) * 512;
    unsigned ssa = L_SST + tk * 8;
    if (B) { PIN(s1a); PIN(vus0); PIN(ssa); PIN(vos); }
    const unsigned ks0 = w * 5 + (w < 3 ? w : 3);
    u32x4 uf[2][6];
    auto u_ld = [&](int tt) AI {
        const unsigned so = WS_U + ((rg * 4 + tt) * 43 + ks0) * 1024;
        u32x4* u = uf[tt & 1];
        u[0] = ld16c<0>(rw, l16, so); u[1] = ld16c<1024>(rw, l16, so); u[2] = ld16c<2048>(rw, l16, so); u[3] = ld16c<3072>(rw, l16, so);
        u[4] = ld16c<0>(rw, l16, so + 4096);
        if (A) u[5] = ld16c<0>(rw, l16, w < 3 ? so + 5120 : OOB);
    };
    f32x4 acc[3], oa[3], pa[3], t1v[3], s1v[3], ev[3]; f32x2 st; unsigned ua[3], ub[3];
    auto silu = [&](auto tc, auto ic) AI {
        CI(tt, tc); CI(i, ic);
        const unsigned xb = xp1 + (tt & 1) * 12288;
        auto Lm = [&](int m) AI { s1v[m] = LD(f32x4, s1a + 768 + m * 64); t1v[m] = LD(f32x4, s1a + m * 64); };
        auto Pm = [&](int m) AI {
#pragma unroll
            for (int r = 0; r < 4; ++r) oa[m][r] = __builtin_fmaf(st.y, __builtin_fmaf(st.x, s1v[m][r], oa[m][r]), t1v[m][r]);
        };
        auto Zm = [&](int m) AI {
#pragma unroll
            for (int r = 0; r < 4; ++r) oa[m][r] = __builtin_fmaf(st.y, pa[m][r], oa[m][r]);
        };
        auto Em = [&](int m) AI {
#pragma unroll
            for (int r = 0; r < 4; ++r) ev[m][r] = __builtin_amdgcn_exp2f(oa[m][r]);
        };
        auto Cm = [&](int m) AI {
#pragma unroll
            for (int r = 0; r < 4; ++r) ev[m][r] = __builtin_amdgcn_rcpf(ev[m][r]);
        };
        auto Fm = [&](int m) AI { oa[m] *= ev[m]; ua[m] = pk2(oa[m][0], oa[m][1]); ub[m] = pk2(oa[m][2], oa[m][3]); };
        if (i == 0) { st = LD(f32x2, ssa + tt * 128); Lm(0); Lm(1); }
        if (i == 3) { Pm(0); }
        if (i == 4) { pa[0] = LD(f32x4, xb); pa[1] = LD(f32x4, xb + 1024); Lm(2); }
        if (i == 5) Pm(1);
        if (i == 6) pa[2] = LD(f32x4, xb + 2048);
        if (i == 7) Zm(0);
        if (i == 8) Em(0);
        if (i == 9) { Zm(1); ev[0] += 1.f; }
        if (i == 10) Cm(0);
        if (i == 11) Em(1);
        if (i == 12) { Fm(0); ev[1] += 1.f; }
        if (i == 13) Cm(1);
        if (i == 14) Pm(2);
        if (i == 15) Fm(1);
        const unsigned so = WS_U + (rg * 4 + tt) * 44032 + (tile0 + hd * 3) * 512;
        if (i == 16) {
            auto ra_ = __builtin_amdgcn_permlane16_swap(ua[0], ua[1], false, false);
            auto rb_ = __builtin_amdgcn_permlane16_swap(ub[0], ub[1], false, false);
            st16s(rw, vus0, (cg < 7 || hd > 0) ? so : OOB, (u32x4){ra_[0], rb_[0], ra_[1], rb_[1]});
            Zm(2);
        }
        if (i == 17) Em(2);
        if (i == 18) ev[2] += 1.f;
        if (i == 19) Cm(2);
        if (i == 20) Fm(2);
        if (i == 21) {
            auto ra_ = __builtin_amdgcn_permlane16_swap(ua[2], ua[2], false, false);
            auto rb_ = __builtin_amdgcn_permlane16_swap(ub[2], ub[2], false, false);
            st16s(rw, vos, hd < 3 ? so + 1024 : OOB, (u32x4){ra_[0], rb_[0], ra_[1], rb_[1]});
        }
    };
    auto fside = [&](auto tc, auto ic) AI {
        CI(tt, tc); CI(i, ic);
        constexpr unsigned AN = ((tt + 1) & 1) * 16384;
        if (tt > 0 && i == 4) { STAMP(57 + tt); BAR(); }
        if (tt == 0 && i == 2) row_read(W2[0], 0);
        if (tt == 1 && i == 22) row_read(W2[1], 32 * K);
        if (B) {
            IF (tt > 0) silu(IC<tt - 1>{}, ic);
            if (tt == 0 && i == 17) VMW(0);
            if (tt == 0 && i == 6) {
                const float sc1 = (tk & 1) ? 1.f : -1.44269504089f;
#pragma unroll
                for (int m = 0; m < 3; ++m) {
                    const unsigned o = L_S1 + (tk & 1) * 768 + (hd * 48 + m * 16 + g * 4) * 4;
                    LD(f32x4, o) = (LD(f32x4, o) + sacc[m]) * sc1;
                }
            }
        } else {
            IF (tt > 0 && i < 3) LD(f32x4, xp1 + ((tt - 1) & 1) * 12288 + i * 1024) = oa[i];
            IF (tt == 0 && i < 16 && !(i & 1)) w2_one(i < 8 ? hd : hd + 4, 1, 32 * K, IC<(i / 2) & 3>{});
            IF (tt == 1 && i < 16 && !(i & 1)) w2_one(i < 8 ? hd : hd + 4, 2, 0, IC<(i / 2) & 3>{});
            IF (tt == 2 && i < 8 && !(i & 1)) w2_one(hd, 5, 64 * K, IC<(i / 2) & 3>{});
            IF (tt == 2 && i >= 8 && i < 16 && !(i & 1)) w2_one(hd + 4, 3, 32 * K, IC<(i / 2) & 3>{});
            IF (tt == 3 && i < 16 && !(i & 1)) w2_one(hd, i < 8 ? 4 : 3, i < 8 ? 32 * K : 0, IC<(i / 2) & 3>{});
            if (tt < 3 && i == 15) { VMW(8); y_put(tt + 1); }
            if (tt < 3 && i == 17) y_stat(tt + 1);
            if (tt < 2 && i == 23) y_ld(tt + 2);
            if (tt == 2 && i == 21) row_read(W2[2], 0);
        }
        if (tt < 3 && i == 18) { STAMP(52 + tt); BAR(); h_rd(0, AN); h_rd(1, AN); h_rd(2, AN); }
        if (tt < 3 && i == 19) { h_rd(3, AN); h_rd(4, AN); h_rd(5, AN); }
        if (tt < 3 && i == 20) h_rd(6, AN);
        if (tt < 3 && i == 23) h_rd(7, AN);
        if (tt == 3 && i == (A ? 20 : 22)) u_ld(0);
        SB();
    };
    sfor<0, 4>([&](auto tc) AI {
        CI(tt, tc);
#pragma unroll
        for (int m = 0; m < 3; ++m) { oa[m] = acc[m]; acc[m] = (f32x4){0, 0, 0, 0}; }
        SB();
        sfor<0, 24>([&](auto ic) AI { CI(i, ic); acc[i % 3] = mfma32(W1[i / 3][i % 3], hf[i / 3], acc[i % 3]); fside(tc, ic); });
        STAMP(25 + tt);
    });

    if (A) {
#pragma unroll
        for (int m = 0; m < 3; ++m) LD(f32x4, xp1 + 12288 + m * 1024) = acc[m];
        VMW(14);
        BAR();
    } else {
#pragma unroll
        for (int m = 0; m < 3; ++m) oa[m] = acc[m];
        sfor<0, 4>([&](auto ic) AI { silu(IC<3>{}, ic); });
        BAR();
        sfor<4, 22>([&](auto ic) AI { silu(IC<3>{}, ic); });
        SB();
    }
    STAMP(29);
    RELANE();
    if (A) { VMW(6); row_read(W2[3], 0); row_read(W2[4], 32 * K); unsigned tb = 64 * K; row_read(W2[5], tb); }
    else { row_read(W2[2], 0); row_read(W2[3], 32 * K); row_read(W2[4], 64 * K); }
    if (B) u_ld(1);
    f32x4 gb;
    if (B) {
        const u32x2 gv = LD(u32x2, L_GT + 128 + fb * 64 + fg * 16 + fe * 8);
        gb = (f32x4){lo16(gv[0]), hi16(gv[0]), lo16(gv[1]), hi16(gv[1])} * (-1.f / 1.44269504089f);
    }
    unsigned pw1 = pw0 ^ 64; PIN(pw0); PIN(pw1); if (B) PIN(pr);
    f32x4 pp[8];
    auto fin_out = [&](auto tc, auto ic) AI {
        CI(tt, tc); CI(i, ic);
        IF (i < 2) {
#pragma unroll
            for (int ww = 4 * i; ww < 4 * i + 4; ++ww) pp[ww] = LD(f32x4, pr + (tt & 1) * 32768 + ww * 4096);
        }
        if (i == 5) pp[0] += pp[1];
        if (i == 6) pp[2] += pp[3];
        if (i == 7) pp[4] += pp[5];
        if (i == 8) pp[6] += pp[7];
        if (i == 9) pp[0] += pp[2];
        if (i == 10) pp[4] += pp[6];
        const u32x2 xp2 = x1p[tt];
        if (i == 11) { pp[0] += pp[4]; }
        if (i == 12) pp[1] = (f32x4){lo16(xp2[0]), hi16(xp2[0]), lo16(xp2[1]), hi16(xp2[1])};
        if (i == 13) pp[0] = gb * pp[0] + pp[1];
        if (i == 14) __builtin_amdgcn_raw_buffer_store_b64((u32x2){pk2(pp[0][0], pp[0][1]), pk2(pp[0][2], pp[0][3])}, rout, (int)fvo, (int)((rg * 4 + tt) * 16384 + cg * 128), 0);
    };
    sfor<0, 4>([&](auto tc) AI {
        CI(tt, tc);
        u32x4* u = uf[tt & 1];
        IF (B && tt > 0) { fin_out(IC<tt - 1>{}, IC<0>{}); fin_out(IC<tt - 1>{}, IC<1>{}); }
        VMW(A ? (tt == 1 || tt == 2 ? 6 : 0) : (tt == 0 ? 7 : tt == 1 ? 5 : tt == 2 ? 6 : 1));
        TIE2(u[0], u[1]); TIE2(u[2], u[3]); TIE1(u[4]); if (A) TIE1(u[5]);
#pragma nounroll
        for (int it = 0; it < SPIN && anyl(umax4(u[4], umax4(u[3], umax4(u[2], umax4(u[1], umax4(u[0], A ? umax4(u[5], 0) : 0))))) == ~0u); ++it) {
            u_ld(tt); VMW(0); TIE2(u[0], u[1]); TIE2(u[2], u[3]); TIE1(u[4]); if (A) TIE1(u[5]);
        }
        STAMP(30 + tt * 2);
        if (A && tt == 0) u_ld(1);
        f32x4 a2[4] = {{0, 0, 0, 0}, {0, 0, 0, 0}, {0, 0, 0, 0}, {0, 0, 0, 0}};
        if (A && w < 3) {
#pragma unroll
            for (int c = 0; c < 4; ++c) a2[c] = mfma32(W2[5][c], __builtin_bit_cast(bf16x8, u[5]), a2[c]);
        }
        SB();
        sfor<0, 20>([&](auto ic) AI {
            CI(i, ic); constexpr int s = i >> 2, c = i & 3;
            a2[c] = mfma32(W2[s][c], __builtin_bit_cast(bf16x8, u[s]), a2[c]);
            IF (B && tt > 0 && i >= 2) fin_out(IC<tt - 1>{}, ic);
            IF (s == 4 && c > 0) LD(f32x4, (tt & 1) * 32768 + ((c - 1) >> 1) * 128 + (((c - 1) & 1) ? pw1 : pw0)) = a2[c - 1];
            SB();
        });
        if (tt < 2) u_ld(tt + 2);
        LD(f32x4, (tt & 1) * 32768 + 128 + pw1) = a2[3];
        BAR();
        STAMP(31 + tt * 2);
    });
    if (B) {
        sfor<0, 15>([&](auto ic) AI { fin_out(IC<3>{}, ic); });
    }
    if (A && b == 0 && w == 0) {
#pragma nounroll
        for (int it = 0; it < SPIN; ++it) {
            u32x4 f0 = ld16c<0>(rw, l16, WS_FLAG), f1_ = ld16c<1024>(rw, l16, WS_FLAG), f2 = ld16c<2048>(rw, l16, WS_FLAG), f3 = ld16c<3072>(rw, l16, WS_FLAG);
            VMW(0); TIE2(f0, f1_); TIE2(f2, f3);
            const u32x4 tg = {tag, tag, tag, tag};
            const u32x4 d = (f0 ^ tg) | (f1_ ^ tg) | (f2 ^ tg) | (f3 ^ tg);
            if (!anyl((d[0] | d[1] | d[2] | d[3]) != 0)) break;
        }
        st4s(rw, lane == 0 ? 0u : OOB, WS_EPOCH, tag);
    }
    STAMP(38); STAMPC(63);
    IF (B) asm volatile(".Lpfe:");
    __builtin_amdgcn_endpgm();
    };
    if (kh == 0) body(IC<0>{});
    body(IC<1>{});
}

#if defined(__HIP_DEVICE_COMPILE__) && !defined(PK)
#pragma clang attribute pop
#endif
extern "C" void kernel_launch(void* const* d_in, const int* in_sizes, int n_in, void* d_out, int out_size, void* d_ws, size_t ws_size, hipStream_t stream) {
    fused_block<<<256, 512, 0, stream>>>((const char*)d_in[0], (const char*)d_in[1], (const char*)d_in[2], (const char*)d_in[4], (const char*)d_in[5],
                                         (const char*)d_in[6], (const char*)d_in[7], (char*)d_ws, (char*)d_out);
}
```

```cpp
#include <hip/hip_runtime.h>
#include <stdint.h>

#if defined(__HIP_DEVICE_COMPILE__) && !defined(PK)
#pragma clang attribute push(__attribute__((target("no-packed-fp32-ops"))), apply_to = function)
#endif
#define LAS __attribute__((address_space(3)))
typedef __bf16 bf16x8 __attribute__((ext_vector_type(8)));
typedef __bf16 bf16x4 __attribute__((ext_vector_type(4)));
typedef __bf16 bf16x2 __attribute__((ext_vector_type(2)));
typedef short s16x4 __attribute__((ext_vector_type(4)));
typedef float f32x4 __attribute__((ext_vector_type(4)));
typedef float f32x2 __attribute__((ext_vector_type(2)));
typedef unsigned u32x4 __attribute__((ext_vector_type(4)));
typedef unsigned u32x2 __attribute__((ext_vector_type(2)));
typedef __amdgpu_buffer_rsrc_t rsrc_t;
#define DI __device__ __forceinline__
#define U2F(x) __builtin_bit_cast(float, (unsigned)(x))
#define F2U(x) __builtin_bit_cast(unsigned, (float)(x))
#define EXP 0
#define PF 13
#define OUTF 0
#define UPK 0
#define PT1 0
#define PT2 3
#define MAP 1
#define ROT 1

constexpr unsigned WS_EPOCH = 0, WS_FLAG = 4096, WS_MODP = 8192, WS_FL2 = 57344, WS_ST = 65536, WS_O = 1u << 20, WS_Y = 3u << 20, WS_U = 5u << 20, WS_BYTES = 11u << 20;
constexpr int SPIN = 1 << 15;
constexpr unsigned K = 1024;
constexpr unsigned L_M = 0, L_STAT = 144 * K, L_RST = 149 * K, L_SC = 150 * K, L_AS = 152 * K, L_MK = 156 * K;
constexpr unsigned L_H = 121 * K, L_X = 97 * K;
constexpr unsigned L_CB = 153 * K, L_GT = 155 * K, L_SST = 155 * K + 512, L_S1 = 156 * K;
constexpr unsigned L_PW = 121 * K, PWD = 16 * K, L_ACT = 97 * K, L_XC1 = 129 * K;
constexpr unsigned Z0 = 0, Z1 = 11 * K, Z2 = 22 * K, Z3 = 33 * K, Z4 = 44 * K, Z5 = 64 * K, Z6 = 75 * K, Z7 = 86 * K, Z8 = 97 * K, Z9 = 108 * K;

DI unsigned pk2(float a, float b) { bf16x2 v = {(__bf16)a, (__bf16)b}; return __builtin_bit_cast(unsigned, v); }
DI float lo16(unsigned u) { return U2F(u << 16); }
DI float hi16(unsigned u) { return U2F(u & 0xffff0000u); }
template <int CTRL> DI float dpp(float v) { return U2F(__builtin_amdgcn_update_dpp(0, (int)F2U(v), CTRL, 0xf, 0xf, true)); }
template <int CTRL> DI unsigned dppu(unsigned v) { return (unsigned)__builtin_amdgcn_update_dpp(0, (int)v, CTRL, 0xf, 0xf, true); }
DI float fmx(float a, float b) { return __builtin_elementwise_maximum(a, b); }
DI float dot2(unsigned a, unsigned b, float c) { return __builtin_amdgcn_fdot2_f32_bf16(__builtin_bit_cast(bf16x2, a), __builtin_bit_cast(bf16x2, b), c, false); }
DI float sw16add(float a, float b) { auto r = __builtin_amdgcn_permlane16_swap(F2U(a), F2U(b), false, false); return U2F(r[0]) + U2F(r[1]); }
DI float sw32add(float a, float b) { auto r = __builtin_amdgcn_permlane32_swap(F2U(a), F2U(b), false, false); return U2F(r[0]) + U2F(r[1]); }
DI float xmax16(float v) { auto r = __builtin_amdgcn_permlane16_swap(F2U(v), F2U(v), false, false); return fmx(U2F(r[0]), U2F(r[1])); }
DI float xmax32(float v) { auto r = __builtin_amdgcn_permlane32_swap(F2U(v), F2U(v), false, false); return fmx(U2F(r[0]), U2F(r[1])); }
DI f32x4 mfma32(bf16x8 a, bf16x8 b, f32x4 c) { return __builtin_amdgcn_mfma_f32_16x16x32_bf16(a, b, c, 0, 0, 0); }
DI f32x4 mfma32u(u32x4 a, u32x4 b, f32x4 c) { return mfma32(__builtin_bit_cast(bf16x8, a), __builtin_bit_cast(bf16x8, b), c); }
DI f32x4 mfma16u(unsigned a0, unsigned a1, unsigned b0, unsigned b1, f32x4 c) {
    return __builtin_amdgcn_mfma_f32_16x16x16bf16_1k(__builtin_bit_cast(s16x4, (u32x2){a0, a1}), __builtin_bit_cast(s16x4, (u32x2){b0, b1}), c, 0, 0, 0);
}
DI rsrc_t mk(const void* p, unsigned n) { return __builtin_amdgcn_make_buffer_rsrc((void*)p, (short)0, (int)n, 0x00020000); }
DI unsigned nosent(unsigned v) { return v < 0xfffffffeu ? v : 0xfffffffeu; }
DI unsigned umax3(unsigned a, unsigned b, unsigned c) { unsigned m = a > b ? a : b; return m > c ? m : c; }
DI unsigned umax4(u32x4 v, unsigned m) { return umax3(umax3(v[0], v[1], m), v[2], v[3]); }

#define BAR() asm volatile("s_waitcnt lgkmcnt(0)\n\ts_barrier" ::: "memory")
#define VMW(n) asm volatile("s_waitcnt vmcnt(%0)" ::"n"(n) : "memory")
#define TIE1(a) asm volatile("; TIE %0" : "+v"(a))
#define TIE2(a, b) asm volatile("; TIE %0 %1" : "+v"(a), "+v"(b))
DI void dma1(rsrc_t r, unsigned m0, unsigned vo, unsigned so) {
    asm volatile("s_mov_b32 m0, %0\n\ts_nop 0\n\tbuffer_load_dwordx4 %1, %2, %3 offen lds" ::"s"(m0), "v"(vo), "s"(r), "s"(so) : "m0", "memory");
}
template <int OFF> DI void dma1o(rsrc_t r, unsigned m0, unsigned vo, unsigned so) {
    asm volatile("s_mov_b32 m0, %0\n\ts_nop 0\n\tbuffer_load_dwordx4 %1, %2, %3 offen offset:%4 lds" ::"s"(m0), "v"(vo), "s"(r), "s"(so), "n"(OFF) : "m0", "memory");
}
DI void touch(unsigned& d, rsrc_t r, unsigned vo, unsigned so) {
    asm volatile("buffer_load_dword %0, %1, %2, %3 offen ; touch" : "+v"(d) : "v"(vo), "s"(r), "s"(so) : "memory");
}
DI void dma3(rsrc_t r, unsigned m0, unsigned vo, unsigned so) {
    asm volatile("s_mov_b32 m0, %0\n\ts_nop 0\n\tbuffer_load_dwordx4 %1, %2, %3 offen lds\n\tbuffer_load_dwordx4 %1, %2, %3 offen offset:1024 lds\n\tbuffer_load_dwordx4 %1, %2, %3 offen offset:2048 lds"
                 ::"s"(m0), "v"(vo), "s"(r), "s"(so) : "m0", "memory");
}
DI void dma3v(rsrc_t r, unsigned m0, unsigned v0, unsigned v1, unsigned v2, unsigned so) {
    asm volatile("s_mov_b32 m0, %0\n\ts_nop 0\n\tbuffer_load_dwordx4 %1, %4, %5 offen lds\n\tbuffer_load_dwordx4 %2, %4, %5 offen offset:1024 lds\n\tbuffer_load_dwordx4 %3, %4, %5 offen offset:2048 lds"
                 ::"s"(m0), "v"(v0), "v"(v1), "v"(v2), "s"(r), "s"(so) : "m0", "memory");
}
DI void dma4(rsrc_t r, unsigned m0, unsigned vo0, unsigned vo1, unsigned so) {
    asm volatile("s_mov_b32 m0, %0\n\ts_nop 0\n\tbuffer_load_dwordx4 %1, %3, %4 offen lds\n\tbuffer_load_dwordx4 %2, %3, %5 offen offset:1024 lds\n\t"
                 "buffer_load_dwordx4 %1, %3, %6 offen offset:2048 lds\n\tbuffer_load_dwordx4 %2, %3, %7 offen offset:3072 lds"
                 ::"s"(m0), "v"(vo0), "v"(vo1), "s"(r), "s"(so), "s"(so + 7168u), "s"(so + 14336u), "s"(so + 21504u) : "m0", "memory");
}
template <int OFF> DI u32x4 ld16(rsrc_t r, unsigned vo, unsigned so) {
    u32x4 v; asm volatile("buffer_load_dwordx4 %0, %1, %2, %3 offen offset:%4" : "=v"(v) : "v"(vo), "s"(r), "s"(so), "n"(OFF) : "memory"); return v;
}
template <int OFF> DI u32x4 ld16c(rsrc_t r, unsigned vo, unsigned so) {
    u32x4 v; asm volatile("buffer_load_dwordx4 %0, %1, %2, %3 offen offset:%4 sc1" : "=v"(v) : "v"(vo), "s"(r), "s"(so), "n"(OFF) : "memory"); return v;
}
DI u32x2 ld8(rsrc_t r, unsigned vo, unsigned so) {
    u32x2 v; asm volatile("buffer_load_dwordx2 %0, %1, %2, %3 offen" : "=v"(v) : "v"(vo), "s"(r), "s"(so) : "memory"); return v;
}
DI unsigned ld4(rsrc_t r, unsigned vo, unsigned so) {
    unsigned v; asm volatile("buffer_load_dword %0, %1, %2, %3 offen" : "=v"(v) : "v"(vo), "s"(r), "s"(so) : "memory"); return v;
}
DI unsigned ld4c(rsrc_t r, unsigned vo, unsigned so) {
    unsigned v; asm volatile("buffer_load_dword %0, %1, %2, %3 offen sc1" : "=v"(v) : "v"(vo), "s"(r), "s"(so) : "memory"); return v;
}
DI void st16c(rsrc_t r, unsigned vo, u32x4 v) { __builtin_amdgcn_raw_buffer_store_b128(v, r, (int)vo, 0, 16); }
DI void st4c(rsrc_t r, unsigned vo, unsigned v) { __builtin_amdgcn_raw_buffer_store_b32(v, r, (int)vo, 0, 16); }
DI void st16s(rsrc_t r, unsigned vo, unsigned so, u32x4 v) { __builtin_amdgcn_raw_buffer_store_b128(v, r, (int)vo, (int)so, 16); asm volatile("s_nop 1" ::"v"(v) : "memory"); }
DI void st16n(rsrc_t r, unsigned vo, unsigned so, u32x4 v) { __builtin_amdgcn_raw_buffer_store_b128(v, r, (int)vo, (int)so, 16); }
DI void st4s(rsrc_t r, unsigned vo, unsigned so, unsigned v) { __builtin_amdgcn_raw_buffer_store_b32(v, r, (int)vo, (int)so, 16); }
#define SB() __builtin_amdgcn_sched_barrier(0)
#define PIN(a) asm volatile("" : "+v"(a))
#define UNLIKELY(c) __builtin_expect(!!(c), 0)
constexpr unsigned OOB = 0x40000000u;
DI unsigned newlane() { unsigned l; asm volatile("v_mbcnt_lo_u32_b32 %0, -1, 0\n\tv_mbcnt_hi_u32_b32 %0, -1, %0" : "=v"(l)); return l; }
DI bool anyl(bool c) { return __builtin_amdgcn_ballot_w64(c) != 0; }

#define STAMP(i)
#define STAMPC(i)

#define XS(i)
template <int N> struct IC { static constexpr int v = N; };
template <int I, int N, class F> DI void sfor_(F& f) { if constexpr (I < N) { f(IC<I>{}); sfor_<I + 1, N>(f); } }
template <int I, int N, class F> DI void sfor(F f) { sfor_<I, N>(f); }
#define AI __attribute__((always_inline))
#define IF(c) if constexpr (c)
#define CI(n, c) constexpr int n = decltype(c)::v
DI void dma4s(rsrc_t r, unsigned m0, unsigned vo, unsigned so, unsigned stride) {
    asm volatile("s_mov_b32 m0, %0\n\ts_nop 0\n\tbuffer_load_dwordx4 %1, %2, %3 offen lds\n\tbuffer_load_dwordx4 %1, %2, %4 offen offset:1024 lds\n\t"
                 "buffer_load_dwordx4 %1, %2, %5 offen offset:2048 lds\n\tbuffer_load_dwordx4 %1, %2, %6 offen offset:3072 lds"
                 ::"s"(m0), "v"(vo), "s"(r), "s"(so), "s"(so + stride - 1024u), "s"(so + 2 * (stride - 1024u)), "s"(so + 3 * (stride - 1024u)) : "m0", "memory");
}

__global__ __launch_bounds__(512) void fused_block(const char* x, const char* wqkv, const char* wo, const char* w1, const char* w2,
                                                   const char* cvec, const char* wmod, char* ws, char* out) {
    __shared__ __attribute__((aligned(16))) char lds[163840];
    LAS char* L = (LAS char*)lds;
    const unsigned LB = (unsigned)(uintptr_t)L;
#define LD(T, off) (*(LAS T*)(L + (off)))
#define TRF(off, d) __builtin_shufflevector(__builtin_amdgcn_ds_read_tr16_b64_v4bf16((LAS bf16x4*)(L + (off))), __builtin_amdgcn_ds_read_tr16_b64_v4bf16((LAS bf16x4*)(L + (off) + (d))), 0, 1, 2, 3, 4, 5, 6, 7)
    unsigned lane = __builtin_amdgcn_workitem_id_x() & 63; const unsigned w = __builtin_amdgcn_readfirstlane(__builtin_amdgcn_workitem_id_x() >> 6);
    const unsigned b = __builtin_amdgcn_workgroup_id_x();
    STAMP(0); STAMPC(62);
    const unsigned rg = ((b & 7) >> 1) * 8 + (b >> 5), cg = (b & 1) * 4 + ((b >> 3) & 3);
    const unsigned rot = rg & 3;
    unsigned r8, c8, tk, g, q, p, l16, vy, swz0, fl, vo1k0, vo1k1, tre, tro, vos;
    auto derive = [&]() AI {
        r8 = lane >> 3; c8 = lane & 7; tk = lane & 15; g = lane >> 4; q = (lane & 15) >> 2; p = lane & 3;
        l16 = lane * 16; vy = r8 * 1024 + c8 * 16; swz0 = (c8 ^ (r8 & 2)) * 16; fl = ((q >> 1) & 1) | ((g & 1) << 1);
        vo1k0 = r8 * 1024 + swz0; vo1k1 = vo1k0 ^ 64;
        const unsigned trb = (8 * g + q) * 128 + (p >> 1) * 16 + (p & 1) * 8 + w * 4096;
        tre = trb + (fl << 1) * 16; tro = trb + (2 ^ (fl << 1)) * 16;
        vos = (g & 1) ? OOB : (g >> 1) * 256 + tk * 16;
    };
    derive();
#define RELANE() do { lane = newlane(); derive(); } while (0)
    const unsigned hd = w & 3, kh = w >> 2;
    const rsrc_t rx = mk(x, 2048 * 1024), rq = mk(wqkv, 512 * 3072), ro = mk(wo, 512 * 1024), r1 = mk(w1, 512 * 2752), r2 = mk(w2, 1376 * 1024);
    const rsrc_t rc = mk(cvec, 1024), rm = mk(wmod, 512 * 6144), rw = mk(ws, WS_BYTES), rout = mk(out, 2048 * 1024);
    const bool prod = b < 192 && w == 0;

    unsigned pfd = 0;
    {
        uint64_t pc; unsigned len;
        asm volatile("s_getpc_b64 %0\n.Lpfb:\n\ts_mov_b32 %1, .Lpfe-.Lpfb\n\ts_max_u32 %1, %1, .Lpfa-.Lpfb" : "=s"(pc), "=s"(len) :: "scc");
        touch(pfd, mk((const void*)pc, len), lane < 4 ? lane * 64 : OOB, ((b >> 3) * 8 + w) * 256);
    }
    unsigned ep = ld4c(rw, 0, WS_EPOCH);
    unsigned cv;
    if (prod) {
        cv = ld4(rc, lane * 4, (b & 3) * 256);
        const unsigned vm0 = r8 * 6144 + swz0, vm1 = vm0 ^ 64;
        sfor<0, 16>([&](auto ic) AI {
            CI(i, ic);
            dma1o<(i & 3) * 1024>(rm, LB + L_M + (i >> 2) * 4096, (i & 1) ? vm1 : vm0, (b & 3) * (128 * 6144) + (b >> 2) * 128 + i * (8 * 6144) - (i & 3) * 1024);
        });
    }
    u32x4 xr[10];
#pragma unroll
    for (int n = 0; n < 10; ++n) {
        int row = (int)rg * 64 - 16 + n * 8; if (row < 0) row += 16;
        xr[n] = ld16<0>(rx, vy, (unsigned)row * 1024 + w * 128);
    }
    constexpr int NT = (PF & 4) ? 1 : 0;
    {
        const char* tp = w == 0 ? wo : w == 1 ? w1 : w == 2 ? w2 : w == 3 ? out + rg * 65536 : w == 4 ? ws + WS_O + rg * 65536 : w == 5 ? ws + WS_Y + rg * 65536 : w == 6 ? ws + WS_U + rg * 176128 : w2 + 1375 * 1024;
        touch(pfd, mk(tp, 4), lane == 0 ? 0u : OOB, 0);
    }
    const unsigned vq = r8 * 3072 + (swz0 ^ ((w & 1) * 64));
    auto qkv_issue = [&](int r, unsigned slot) AI {
        const unsigned ks = kh * 8 + ((r + 2 * rot) & 7);
        dma3(rq, LB + slot + kh * 12288 + hd * 3072, vq, (ks * 32 + hd * 8) * 3072 + cg * 128);
    };
    STAMP(1);
    if (prod) {
        VMW(26 + NT); TIE2(ep, cv);
        const float a = lo16(cv), c = hi16(cv);
        LD(unsigned, L_SC + lane * 4) = pk2(a * __builtin_amdgcn_rcpf(1.f + __builtin_amdgcn_exp2f(-1.44269504089f * a)), c * __builtin_amdgcn_rcpf(1.f + __builtin_amdgcn_exp2f(-1.44269504089f * c)));
        VMW(10 + NT);
        f32x4 acc[4] = {{0, 0, 0, 0}, {0, 0, 0, 0}, {0, 0, 0, 0}, {0, 0, 0, 0}};
#pragma unroll
        for (int s = 0; s < 4; ++s) {
            const bf16x8 sv = LD(bf16x8, L_SC + s * 64 + g * 16);
            acc[0] = mfma32(TRF(L_M + s * 4096 + tre, 512), sv, acc[0]); acc[1] = mfma32(TRF(L_M + s * 4096 + tro, 512), sv, acc[1]);
            acc[2] = mfma32(TRF(L_M + s * 4096 + (tre ^ 64), 512), sv, acc[2]); acc[3] = mfma32(TRF(L_M + s * 4096 + (tro ^ 64), 512), sv, acc[3]);
        }
        u32x4 v;
#pragma unroll
        for (int j = 0; j < 4; ++j) {
            const float e = (tk & 2) ? ((tk & 1) ? acc[3][j] : acc[2][j]) : ((tk & 1) ? acc[1][j] : acc[0][j]);
            v[j] = __builtin_amdgcn_perm(F2U(e), ep + 0x01010101u, 0x07060500u | j);
        }
        st16s(rw, tk < 4 ? tk * 64 + g * 16 : OOB, WS_MODP + b * 256, v);
    } else if (w == 4) {
        const float NI = -__builtin_inff();
        f32x4 mcur, mprev; bool vc[4], vp[4];
#pragma unroll
        for (int r = 0; r < 4; ++r) {
            const int d = (int)tk - (int)(4 * g + r); vc[r] = d >= 0 && !(d & 1); vp[r] = d <= -2 && !(d & 1);
            mcur[r] = vc[r] ? 0.f : NI; mprev[r] = vp[r] ? 0.f : NI;
        }
        const u32x4 M = {(vp[0] ? 0xffffu : 0u) | (vp[1] ? 0xffff0000u : 0u), (vp[2] ? 0xffffu : 0u) | (vp[3] ? 0xffff0000u : 0u),
                         (vc[0] ? 0xffffu : 0u) | (vc[1] ? 0xffff0000u : 0u), (vc[2] ? 0xffffu : 0u) | (vc[3] ? 0xffff0000u : 0u)};
        LD(f32x4, L_MK + l16) = mcur; LD(f32x4, L_MK + 1024 + l16) = mprev; LD(u32x4, L_MK + 2048 + l16) = M;
        LD(u32x4, L_MK + 3072 + l16) = (u32x4){0x3f803f80u, 0x3f803f80u, 0x3f803f80u, 0x3f803f80u};
    }
    STAMP(2);
    qkv_issue(0, 48 * K); qkv_issue(1, 72 * K);
    float t[10][8];
#pragma unroll
    for (int h5 = 0; h5 < 2; ++h5) {
        if (h5 == 0) { VMW(11 + NT); } else { VMW(9 + NT); }
        float s[5], qq[5];
#pragma unroll
        for (int n = 0; n < 5; ++n) {
            TIE1(xr[h5 * 5 + n]);
            const u32x4 v = xr[h5 * 5 + n];
            s[n] = dot2(v[3], 0x3f803f80u, dot2(v[2], 0x3f803f80u, dot2(v[1], 0x3f803f80u, dot2(v[0], 0x3f803f80u, 0.f))));
            qq[n] = dot2(v[3], v[3], dot2(v[2], v[2], dot2(v[1], v[1], dot2(v[0], v[0], 0.f))));
        }
#define A5(F) F(0) F(1) F(2) F(3) F(4)
#define ST_A(n) "v_add_f32_dpp %" #n ", %" #n ", %" #n " row_half_mirror row_mask:0xf bank_mask:0x5\n\t"
#define ST_C(n) "v_add_f32_dpp %" #n ", %" #n ", %" #n " quad_perm:[1,0,3,2] row_mask:0xf bank_mask:0xf\n\t"
#define ST_D(n) "v_add_f32_dpp %" #n ", %" #n ", %" #n " quad_perm:[2,3,0,1] row_mask:0xf bank_mask:0xf\n\t"
#define ST_B(n, m) "v_add_f32_dpp %" #n ", %" #m ", %" #m " row_half_mirror row_mask:0xf bank_mask:0xa\n\t"
        asm volatile("s_nop 1\n\t" A5(ST_A) ST_B(0, 5) ST_B(1, 6) ST_B(2, 7) ST_B(3, 8) ST_B(4, 9) A5(ST_C) A5(ST_D)
                     : "+v"(s[0]), "+v"(s[1]), "+v"(s[2]), "+v"(s[3]), "+v"(s[4]) : "v"(qq[0]), "v"(qq[1]), "v"(qq[2]), "v"(qq[3]), "v"(qq[4]));
        unsigned sa = L_STAT + r8 * 64 + w * 8 + (c8 >> 2) * 4; PIN(sa);
#pragma unroll
        for (int n = 0; n < 5; ++n) LD(float, sa + (h5 * 5 + n) * 512) = s[n];
        if (h5 == 0) qkv_issue(2, 120 * K); else qkv_issue(3, 96 * K);
    }
    TIE1(ep);
    const unsigned tag = ep + 0x01010101u;
    auto mod_ld = [&](unsigned g0, unsigned g1, u32x4& a, u32x4& c) AI {
        a = ld16c<0>(rw, l16, WS_MODP + g0 * 1024); c = ld16c<0>(rw, l16, WS_MODP + g1 * 1024);
    };
    auto mod_bad = [&](u32x4 a, u32x4 c) AI {
        const unsigned ta = __builtin_amdgcn_perm(__builtin_amdgcn_perm(a[3], a[2], 0x0c0c0400u), __builtin_amdgcn_perm(a[1], a[0], 0x0c0c0400u), 0x05040100u);
        const unsigned tc = __builtin_amdgcn_perm(__builtin_amdgcn_perm(c[3], c[2], 0x0c0c0400u), __builtin_amdgcn_perm(c[1], c[0], 0x0c0c0400u), 0x05040100u);
        return anyl(ta != tag || tc != tag);
    };
    auto mod_fin = [&](unsigned g0, unsigned g1, u32x4 a, u32x4 c, float add1) AI {
        TIE2(a, c);
#pragma nounroll
        for (int it = 0; it < SPIN && mod_bad(a, c); ++it) { mod_ld(g0, g1, a, c); VMW(0); TIE2(a, c); }
        float s[4];
#pragma unroll
        for (int j = 0; j < 4; ++j) s[j] = sw16add(U2F(a[j] & 0xffffff00u), U2F(c[j] & 0xffffff00u));
        const unsigned r = pk2(sw32add(s[0], s[2]), sw32add(s[1], s[3]));
        const float ad = (g & 1) ? add1 : 0.f;
        return pk2(lo16(r) + ad, hi16(r) + ad);
    };
    auto unpack = [&](int n0, int n1) AI {
#pragma unroll
        for (int n = n0; n < n1; ++n)
#pragma unroll
            for (int j = 0; j < 4; ++j) { t[n][2 * j] = lo16(xr[n][j]); t[n][2 * j + 1] = hi16(xr[n][j]); PIN(t[n][2 * j]); PIN(t[n][2 * j + 1]); }
    };
    STAMP(3);
    BAR();
    STAMP(4);
    if (w == 4 || w == 5) {
        const unsigned row = w == 4 ? lane : 64 + tk;
        const f32x4 a = LD(f32x4, L_STAT + row * 64) + LD(f32x4, L_STAT + row * 64 + 16) + LD(f32x4, L_STAT + row * 64 + 32) + LD(f32x4, L_STAT + row * 64 + 48);
        const float mu = (a[0] + a[2]) * (1.f / 512.f);
        const float var = __builtin_fmaxf((a[1] + a[3]) * (1.f / 512.f) - mu * mu, 0.f);
        const float rs = __builtin_amdgcn_rsqf(var + 1e-5f);
        LD(f32x2, L_RST + row * 8) = (f32x2){rs, -mu * rs};
    }
    qkv_issue(4, 0);
    unpack(UPK, (UPK + 10) / 2);
    qkv_issue(5, 24 * K);
    unpack((UPK + 10) / 2, 10);
    u32x4 ma, mc;
    mod_ld(w, 8 + w, ma, mc);
    VMW(11); TIE1(pfd); BAR();
    STAMP(5);
    bf16x8 Wq[8][3];
    const unsigned trq = kh * 12288 + g * 3072 + q * 128 + (((2 * hd) ^ (fl << 1)) + (p >> 1)) * 16 + (p & 1) * 8;
    unsigned trqh = trq + 72 * K; PIN(trqh);
    auto qkv_read = [&](int r, unsigned base, unsigned slot) AI {
#pragma unroll
        for (int m = 0; m < 3; ++m) Wq[r][m] = TRF(base + slot + m * 1024, 512);
    };
    unsigned ra = L_RST + r8 * 8; PIN(ra);
    auto nrm = [&](int n) AI {
        const f32x2 st = LD(f32x2, ra + n * 64);
#pragma unroll
        for (int j = 0; j < 8; ++j) { t[n][j] = __builtin_fmaf(t[n][j], st.x, st.y); PIN(t[n][j]); }
    };
    qkv_read(0, trq, 48 * K); qkv_read(1, trqh, 0); qkv_read(2, trqh, 48 * K);
    nrm(0); nrm(1);
    BAR();
    STAMP(6);
    qkv_issue(6, 48 * K);
    nrm(2); nrm(3); nrm(4); nrm(5);
    qkv_issue(7, 72 * K);
    nrm(6); nrm(7); nrm(8); nrm(9);
    const unsigned tile0 = cg * 11 > 75 ? 75 : cg * 11;
    const unsigned hrot = (hd - rot) & 3;
    STAMP(7);
    auto body = [&](auto KHC) AI {
    constexpr bool A = decltype(KHC)::v == 0, B = !A;
    constexpr unsigned KH = decltype(KHC)::v;
    if (B) __builtin_amdgcn_s_setprio(1);
    bf16x8 Wo[4][4], W1[8][3], W2[6][4];
    unsigned v1o[3];
    const unsigned f1 = hd == 3 ? 8 : 3 * hd;
    auto v1o_mk = [&]() AI {
#pragma unroll
        for (int j = 0; j < 3; ++j) { const unsigned ch = (f1 + j) * 64 + lane, r = ch / 22; v1o[j] = r * 2752 + (ch - r * 22) * 16 - j * 1024; PIN(v1o[j]); }
    };
    auto w1_issue = [&](int h2, int r, unsigned slot) AI {
        dma3v(r1, LB + slot + f1 * 1024, v1o[0], v1o[1], v1o[2], (h2 * 8 + ((r + 2 * rot) & 7)) * (32 * 2752) + tile0 * 32);
    };
    auto w1_one = [&](int h2, int r, unsigned slot, auto jc) AI {
        CI(j, jc);
        dma1o<j * 1024>(r1, LB + slot + f1 * 1024, v1o[j], (h2 * 8 + ((r + 2 * rot) & 7)) * (32 * 2752) + tile0 * 32);
    };
    auto wo_one = [&](int r, auto jc) AI {
        CI(j, jc);
        dma1o<j * 1024>(ro, LB + r * 16384 + hd * 4096, (j & 1) ? vo1k1 : vo1k0, (hd * 4 + r) * 32768 + cg * 128 + j * 7168);
    };
    auto row_read = [&](bf16x8* W, unsigned base) AI {
        W[0] = TRF(base + tre, 512); W[1] = TRF(base + tro, 512); W[2] = TRF(base + (tre ^ 64), 512); W[3] = TRF(base + (tro ^ 64), 512);
    };
    unsigned tr1;
    auto w1_read = [&](int r, unsigned slot) AI {
        unsigned ba = tr1; if (slot >= 64 * K) { ba += 64 * K; PIN(ba); slot -= 64 * K; }
#pragma unroll
        for (int m = 0; m < 3; ++m) W1[r][m] = TRF(ba + slot + m * 32, 5632);
    };
    unsigned aw0 = L_H + ((2 * (KH * 4 + hrot) + (c8 >> 2)) * 64 + (c8 & 3) * 16 + (r8 ^ ((c8 & 3) | ((c8 >> 2) * 12)))) * 16, aw1 = aw0 ^ 128;
    unsigned ar0 = L_H + (KH * 512 + g * 16 + (tk ^ g)) * 16, ar1 = L_H + (KH * 512 + 64 + g * 16 + (tk ^ (g | 12))) * 16;
    unsigned xp = L_X + hd * 3072 + l16;
    PIN(aw0); PIN(aw1); PIN(ar0); PIN(ar1); PIN(xp);
    auto pref = [&](int n) AI {
        const unsigned j = b >> 3, hf_ = b & 1;
        if (n == 0) touch(pfd, r1, tk < 12 ? g * 2752 + tk * 128 : OOB, (j * 16 + hd * 4) * 2752 + hf_ * 1344);
        else touch(pfd, r2, (lane >> 2) * 1024 + p * 128, hd < 3 ? (j * 43 + hd * 16) * 1024 + hf_ * 512 : OOB);
    };
    VMW(6);
    STAMP(8);
    f32x4 A0, A1, S0, S1;
    {
        const unsigned r = mod_fin(w, 8 + w, ma, mc, 1.f);
        LD(f32x2, L_AS + w * 512 + (g & 1) * 256 + tk * 16 + (g >> 1) * 8) = (f32x2){lo16(r), hi16(r)};
        unsigned aa = L_AS + w * 512 + c8 * 32; PIN(aa);
        S0 = LD(f32x4, aa); S1 = LD(f32x4, aa + 16); A0 = LD(f32x4, aa + 256); A1 = LD(f32x4, aa + 272);
    }
    STAMP(9);
    u32x4 hw;
    auto norm1 = [&](auto ic) AI {
        constexpr int n = decltype(ic)::v >> 2, j = decltype(ic)::v & 3;
        const f32x4 Av = j < 2 ? A0 : A1, Sv = j < 2 ? S0 : S1;
        hw[j] = pk2(__builtin_fmaf(t[n][2 * j], Av[(2 * j) & 3], Sv[(2 * j) & 3]), __builtin_fmaf(t[n][2 * j + 1], Av[(2 * j + 1) & 3], Sv[(2 * j + 1) & 3]));
        IF (j == 3) LD(u32x4, ((n >> 1) & 1) * 16384 + ((n & 1) ? aw1 : aw0)) = hw;
    };
    sfor<0, 8>(norm1);
    BAR();
    STAMP(10);
    bf16x8 hf[8];
    auto h_rd = [&](int s, unsigned base) AI { hf[s] = LD(bf16x8, base + (s >> 1) * 2048 + ((s & 1) ? ar1 : ar0)); };

    f32x4 aq, ak, av, oq, ok, ov;
    u32x2 kc, vc, kp, vp;
    f32x4 pq_, pk_, pv_, mcur, mprev, sp, sc, sm, ao; u32x4 M, ones, P; unsigned q0, q1, mkp; float t0, t1, mx, inv;
    auto attn = [&](auto Tc, auto ic) AI {
        CI(T, Tc); CI(i, ic);
        const unsigned xb = xp + (T & 1) * 12288;
        constexpr float C = 0.25f * 1.44269504089f;
        if (T == 0) {
            if (i == 0) { pk_ = LD(f32x4, xb + 1024); pv_ = LD(f32x4, xb + 2048); }
            if (i == 5) kc = (u32x2){pk2(ok[0] + pk_[0], ok[1] + pk_[1]), pk2(ok[2] + pk_[2], ok[3] + pk_[3])};
            if (i == 7) vc = (u32x2){pk2(ov[0] + pv_[0], ov[1] + pv_[1]), pk2(ov[2] + pv_[2], ov[3] + pv_[3])};
            return;
        }
        if (i == 0) { pq_ = LD(f32x4, xb); mprev = LD(f32x4, mkp + 1024); pk_ = LD(f32x4, xb + 1024); mcur = LD(f32x4, mkp); pv_ = LD(f32x4, xb + 2048); kp = kc; vp = vc; }
        if (i == 4) {
            if (T == 1) { const float ni = rg == 0 ? -__builtin_inff() : 0.f; mprev += ni; }
            oq += pq_; q0 = pk2(oq[0], oq[1]); q1 = pk2(oq[2], oq[3]); sp = mfma16u(kp[0], kp[1], q0, q1, mprev);
        }
        if (i == 5) { ok += pk_; kc = (u32x2){pk2(ok[0], ok[1]), pk2(ok[2], ok[3])}; sc = mfma16u(kc[0], kc[1], q0, q1, mcur); M = LD(u32x4, mkp + 2048); ones = LD(u32x4, mkp + 3072); }
        if (i == 6) { ov += pv_; vc = (u32x2){pk2(ov[0], ov[1]), pk2(ov[2], ov[3])}; }
        if (i == 7) { t0 = fmx(fmx(fmx(sp[0], sp[1]), sc[0]), sc[1]); t1 = fmx(fmx(fmx(sp[2], sp[3]), sc[2]), sc[3]); }
        if (i == 8) mx = xmax16(fmx(t0, t1));
        if (i == 9) mx = xmax32(mx) * C;
        if (i == 10) { t0 = __builtin_amdgcn_exp2f(__builtin_fmaf(t0, C, -mx)); t1 = __builtin_amdgcn_exp2f(__builtin_fmaf(t1, C, -mx)); }
        if (i == 11) { q0 = pk2(t0, t0); q1 = pk2(t1, t1); P = (u32x4){q0 & M[0], q1 & M[1], q0 & M[2], q1 & M[3]}; }
        if (i == 12) { sm = mfma32u(ones, P, (f32x4){0, 0, 0, 0}); ao = mfma32u((u32x4){vp[0], vp[1], vc[0], vc[1]}, P, (f32x4){0, 0, 0, 0}); }
        if (i == 14) inv = __builtin_amdgcn_rcpf(sm[0]);
        if (i == 15) { ao *= inv; q0 = pk2(ao[0], ao[1]); q1 = pk2(ao[2], ao[3]); }
        if (i == 16) {
            auto ra_ = __builtin_amdgcn_permlane16_swap(q0, q0, false, false);
            auto rb_ = __builtin_amdgcn_permlane16_swap(q1, q1, false, false);
            st16s(rw, vos, WS_O + ((rg * 4 + T - 1) * 16 + cg * 2) * 1024 + hd * 512, (u32x4){ra_[0], rb_[0], ra_[1], rb_[1]});
        }
    };
    const u32x4 ff = {~0u, ~0u, ~0u, ~0u};
    auto clr = [&](int n) AI {
        const unsigned vw = n < 6 ? hd : hd + 4; const int i = n < 6 ? n : n - 6;
        const unsigned ub = WS_U + (rg * 4 + (vw >> 1)) * 44032 + tile0 * 512 + (vw & 1) * 3072;
        const unsigned lo32 = lane < 32 ? 0u : OOB;
        if (i == 0) st16s(rw, l16, WS_O + ((rg * 4 + (vw >> 1)) * 16 + cg * 2 + (vw & 1)) * 1024, ff);
        if (i == 1) st16s(rw, vy, WS_Y + (rg * 64 + vw * 8) * 1024 + cg * 128, ff);
        if (i == 2) st16s(rw, l16, (cg < 7 || (vw & 1)) ? ub : OOB, ff);
        if (i == 3) st16s(rw, l16, ub + 1024, ff);
        if (i == 4) st16s(rw, l16 | ((vw & 1) ? lo32 : 0u), ub + 2048, ff);
        if (i == 5) st16s(rw, vy | lo32, vw == 0 ? WS_ST + (rg * 32 + cg) * 128 : OOB, ff);
    };
    u32x4 m2a, m2c, m3a, m3c, m4a, m4c; unsigned flg;
    u32x4 ofb[2][4], fa;
    const unsigned tg0 = b == 0 ? tag : 0u;
    auto fl_ld = [&]() AI { flg = ld4c(rw, (lane & 31) * 4, WS_FLAG + rg * 128); fa = ld16c<0>(rw, l16, b == 0 ? WS_FLAG + hd * 1024 : OOB); };
    auto o_ld = [&](int tt) AI {
        const unsigned so = WS_O + ((rg * 4 + tt) * 16 + 4 * hd) * 1024;
        u32x4* of = ofb[tt & 1];
        of[0] = ld16c<0>(rw, l16, so); of[1] = ld16c<1024>(rw, l16, so); of[2] = ld16c<2048>(rw, l16, so); of[3] = ld16c<3072>(rw, l16, so);
    };
    auto cbw = [&]() AI { return (g & 1) * 1024 + (tk >> 3) * 64 + (tk & 3) * 16 + ((tk >> 2) & 1) * 8 + (g >> 1) * 4; };
    u32x2 xres[4]; unsigned fvo;
    auto qside = [&](auto Tc, auto ic) AI {
        CI(T, Tc); CI(i, ic);
        constexpr unsigned HN = ((T + 1) & 1) * 16384;
        constexpr int BI = T == 0 ? 7 : 4;
        IF (T == 0 && i < 2) { norm1(IC<8 + 2 * i>{}); norm1(IC<9 + 2 * i>{}); }
        IF (T == 0 && i >= 2 && i < 14) norm1(IC<10 + i>{});
        IF (T == 1 && i >= 6 && i < 22 && !(i & 1)) norm1(IC<24 + (i - 6) / 2>{});
        IF (T == 2 && i < 3) norm1(IC<32 + i>{});
        IF (T == 2 && i >= 5 && i < 8) norm1(IC<30 + i>{});
        IF (T == 2 && A && (i == 8 || i == 9)) norm1(IC<30 + i>{});
        IF (T == 2 && B && (i == 21 || i == 22)) norm1(IC<17 + i>{});
        if (A) {
            const unsigned xb = xp + ((T - 1) & 1) * 12288;
            if (T > 1 && i == 0) LD(f32x4, xb) = oq;
            if (T > 0 && i == 1) LD(f32x4, xb + 1024) = ok;
            if (T > 0 && i == 2) LD(f32x4, xb + 2048) = ov;
            IF (T == 1 && i % 3 == 1) wo_one(i / 12, IC<(i / 3) & 3>{});
            IF (T == 2 && i < 16 && !(i & 1)) wo_one(2 + i / 8, IC<(i / 2) & 3>{});
            IF (T == 2 && i >= 16 && i < 22 && !(i & 1)) w1_one(1, 0, Z5, IC<(i - 16) / 2>{});
            if (T == 2 && i == 3) { VMW(6); row_read(Wo[0], 0); }
            if (T == 2 && i == 11) { v1o_mk(); }
            if (T == 2 && i == 13) { VMW(7); row_read(Wo[1], 16384); }
            IF (T == 3 && i < 18 && !(i & 1)) w1_one(i < 12 ? 1 : 0, i < 6 ? 1 : i < 12 ? 2 : 0, i < 6 ? Z6 : i < 12 ? Z7 : Z0, IC<(i / 2) % 3>{});
            if (T == 3 && i == 1) { VMW(10); row_read(Wo[2], 32768); }
            if (T == 3 && i == 9) { VMW(10); row_read(Wo[3], 49152); }
            IF (T == 4 && i < 6 && !(i & 1)) w1_one(0, 1, Z1, IC<i / 2>{});
            if (T == 4 && i == 4) VMW(3);
            if (T == 4 && i == 5) fl_ld();
            IF (T == 4 && i >= 7 && i < 16) w1_one(0, 2 + (i - 7) / 3, i < 10 ? Z2 : i < 13 ? Z3 : Z4, IC<(i - 7) % 3>{});
            if (T == 4 && i == 6) w1_read(0, Z0);
            if (T == 4 && i == 21) o_ld(1);
            if (T == 4 && i == 18) {
                VMW(9); TIE2(flg, fa);
#pragma nounroll
                for (int it = 0; it < SPIN && anyl(((flg ^ tag) | (fa[0] ^ tg0) | (fa[1] ^ tg0) | (fa[2] ^ tg0) | (fa[3] ^ tg0)) != 0); ++it) { fl_ld(); VMW(0); TIE2(flg, fa); }
                o_ld(0);
            }
            if (T == 0 && i == 7) VMW(0);
            if (T == 2 && i == 22) mod_ld(16 + cg, 40 + cg, m3a, m3c);
            if (T == 3 && i == 13) { VMW(7); const unsigned r = mod_fin(16 + cg, 40 + cg, m3a, m3c, 0.f); LD(unsigned, L_GT + cbw() - (g & 1) * 896) = r; }
            if (T == 3 && i == 15) mod_ld(28 + hd, 36 + hd, m4a, m4c);
            if (T == 4 && i == 16) LD(unsigned, L_CB + 512 + hrot * 128 + cbw()) = mod_fin(28 + hd, 36 + hd, m4a, m4c, 1.f);
        } else {
            if (T == 0 && i == 0) clr(0);
            if (T == 0 && i == 3) clr(6);
            if (T == 0 && i == 6) clr(1);
            if (T == 0 && i == 9) clr(7);
            if (T == 0 && i == 12) clr(5);
            if (T == 0 && i == 7) VMW(3);
            if (T >= 2 && i == 6) clr(T);
            if (T >= 2 && i == 17) clr(T + 6);
            if (T == 1 && i == 7) LD(unsigned, L_CB + hrot * 128 + cbw()) = mod_fin(24 + hd, 32 + hd, m2a, m2c, 1.f);
            if ((PF & 2) && T == PT1 && i == 9) pref(0);
            if ((PF & 8) && T == PT2 && i == 11) pref(1);
            if (T == 2 && i == 3) { VMW(0); if (PT2 < 2) TIE1(pfd); st4s(rw, lane == 0 ? 0u : OOB, WS_FLAG + rg * 128 + cg * 16 + hd * 4, tag); }
            IF (T > 0 && i > 4) attn(IC<T - 1>{}, IC<i - 4>{});
            if (T == 4 && i == 6) {
                fvo = (hd * 4 + g) * 1024 + ((lane >> 3) & 1) * 64 + (lane & 1) * 32 + ((lane >> 1) & 3) * 8; PIN(fvo);
#pragma unroll
                for (int tt = 0; tt < 4; ++tt) xres[tt] = ld8(rx, fvo, (rg * 64 + tt * 16) * 1024 + cg * 128);
            }
            if (T == 4 && i == 13) w1_read(0, Z5);
            if (T == 4 && i == 17) w1_read(1, Z6);
            if (T == 4 && i == 21) w1_read(2, Z7);
        }
        if (i == BI) {
            STAMP(40 + 2 * T);
            BAR();
            STAMP(41 + 2 * T);
            IF (B && T > 0) attn(IC<T - 1>{}, IC<0>{});
        }
        IF (T == 0) {
            if (i == 0) { qkv_read(4, trq, 0); }
            if (i == 2) { qkv_read(5, trq, 24 * K); }
            if (i == 7) { qkv_read(6, trq, 48 * K); }
            if (i == 8) { qkv_read(7, trqh, 0); h_rd(0, HN); h_rd(1, HN); }
            if (i == 9) { h_rd(2, HN); h_rd(3, HN); h_rd(4, HN); }
            if (i == 11) h_rd(5, HN);
            if (i == 13) h_rd(6, HN);
            if (i == 15) h_rd(7, HN);
        }
        IF (T > 0 && T < 4) {
            if (i == 5) h_rd(0, HN);
            if (i >= 5 && i % 3 == 2) h_rd(i / 3, HN);
        }
        SB();
    };
    if (B) { mkp = L_MK + l16; PIN(mkp); PIN(vos); mod_ld(24 + hd, 32 + hd, m2a, m2c); }
    tr1 = (4 * g + q) * 352 + hd * 96 + p * 8; PIN(tr1);
    h_rd(0, 0); h_rd(1, 0); h_rd(2, 0); h_rd(3, 0); qkv_read(3, trqh, 24 * K); h_rd(4, 0); h_rd(5, 0); h_rd(6, 0); h_rd(7, 0);
    sfor<0, 5>([&](auto Tc) AI {
        CI(T, Tc);
        oq = aq; ok = ak; ov = av;
        aq = (f32x4){0, 0, 0, 0}; ak = aq; av = aq;
        SB();
        sfor<0, 8>([&](auto sc_) AI {
            CI(s, sc_);
            IF (T > 0) { aq = mfma32(Wq[s][0], hf[s], aq); qside(Tc, IC<3 * s>{}); }
            ak = mfma32(Wq[s][1], hf[s], ak); qside(Tc, IC<(T > 0 ? 3 * s + 1 : 2 * s)>{});
            av = mfma32(hf[s], Wq[s][2], av); qside(Tc, IC<(T > 0 ? 3 * s + 2 : 2 * s + 1)>{});
        });
        STAMP(11 + T);
    });
    if (A) { LD(f32x4, xp) = aq; LD(f32x4, xp + 1024) = ak; LD(f32x4, xp + 2048) = av; }
    oq = aq; ok = ak; ov = av;
    BAR();

    STAMP(56);
    if (B) {
        sfor<0, 17>([&](auto ic) AI { attn(IC<4>{}, ic); });
        if (b == 0 && w == 4) st4s(rw, lane == 0 ? 0u : OOB, WS_EPOCH, tag);
        SB();
    }
    STAMP(50);
    RELANE();
    const unsigned fb = (lane >> 3) & 1, fg = (lane >> 1) & 3, fe = lane & 1, ftok = hd * 4 + g;
    unsigned pw0 = w * 4096 + tk * 256 + (g ^ (tk & 7)) * 16;
    unsigned pr = ftok * 256 + ((fb * 8 + fe * 4 + fg) ^ (ftok & 7)) * 16;
    auto w2_issue = [&](unsigned vw, int s, unsigned slot) AI {
        const unsigned ks = vw * 5 + (vw < 3 ? vw : 3) + s;
        dma4(r2, LB + slot + vw * 4096, vo1k0, vo1k1, ks * 32768 + cg * 128);
    };
    auto w2_one = [&](unsigned vw, int s, unsigned slot, auto jc) AI {
        CI(j, jc);
        const unsigned ks = vw * 5 + (vw < 3 ? vw : 3) + s;
        dma1o<j * 1024>(r2, LB + slot + vw * 4096, (j & 1) ? vo1k1 : vo1k0, ks * 32768 + cg * 128 + j * 7168);
    };
    u32x2 x1p[4];
    f32x4 sacc[3] = {{0, 0, 0, 0}, {0, 0, 0, 0}, {0, 0, 0, 0}};
    auto nof = [&](auto) AI {};
    auto s1_part = [&](auto s0c, auto s1c, auto side) AI {
        CI(s0, s0c); CI(s1, s1c);
        unsigned ca = L_CB + (tk & 1) * 1024 + KH * 512 + g * 16; PIN(ca);
        bf16x8 yf[8];
#pragma unroll
        for (int s = s0; s < s1; ++s) yf[s] = LD(bf16x8, ca + s * 64);
        sfor<s0, s1>([&](auto sc_) AI {
            CI(s, sc_);
            side(IC<s - s0>{});
#pragma unroll
            for (int m = 0; m < 3; ++m) sacc[m] = mfma32(W1[s][m], yf[s], sacc[m]);
            SB();
        });
    };
    u32x4 yv[4], sv;
    auto y_ld = [&](int tt) AI {
        const unsigned so = WS_Y + (rg * 4 + tt) * 16384 + hd * 128;
        yv[0] = ld16c<0>(rw, vy, so); yv[1] = ld16c<0>(rw, vy, so + 8192); yv[2] = ld16c<512>(rw, vy, so); yv[3] = ld16c<512>(rw, vy, so + 8192);
        sv = ld16c<0>(rw, l16, hd == (unsigned)tt ? WS_ST + (rg * 4 + tt) * 1024 : OOB);
    };
    aw0 += L_ACT - L_H; aw1 += L_ACT - L_H; ar0 += L_ACT - L_H; ar1 += L_ACT - L_H; PIN(ar0); PIN(ar1);
    if (A) { PIN(aw0); PIN(aw1); }
    auto y_put = [&](int tt) AI {
        TIE2(yv[0], yv[1]); TIE2(yv[2], yv[3]); TIE1(sv);
#pragma nounroll
        for (int it = 0; it < SPIN && anyl(umax4(sv, umax4(yv[3], umax4(yv[2], umax4(yv[1], umax4(yv[0], 0))))) == ~0u); ++it) {
            y_ld(tt); VMW(0); TIE2(yv[0], yv[1]); TIE2(yv[2], yv[3]); TIE1(sv);
        }
        LD(u32x4, (tt & 1) * 16384 + aw0) = yv[0]; LD(u32x4, (tt & 1) * 16384 + aw1) = yv[1];
        LD(u32x4, (tt & 1) * 16384 + 8192 + aw0) = yv[2]; LD(u32x4, (tt & 1) * 16384 + 8192 + aw1) = yv[3];
    };
    auto y_stat = [&](int tt) AI {
        if (hd == (unsigned)tt) {
            float s = sw16add(U2F(sv[0]), U2F(sv[2])), qq = sw16add(U2F(sv[1]), U2F(sv[3]));
            s += dpp<0x128>(s); qq += dpp<0x128>(qq);
            s = sw32add(s, s); qq = sw32add(qq, qq);
            const float mu = s * (1.f / 512.f);
            const float var = __builtin_fmaxf(qq * (1.f / 512.f) - mu * mu, 0.f);
            LD(f32x2, L_SST + tt * 128 + (c8 * 2 + (g & 1)) * 8) = (f32x2){-mu, -1.44269504089f * __builtin_amdgcn_rsqf(var + 1e-5f)};
        }
    };
    if (A) {
        unsigned pa0 = L_PW + pw0, pa1 = pa0 ^ 64; PIN(pa0); PIN(pa1);
        auto wo_step = [&](int tt, auto side) AI {
            u32x4* of = ofb[tt & 1];
            TIE2(of[0], of[1]); TIE2(of[2], of[3]);
#pragma nounroll
            for (int it = 0; it < SPIN && anyl(umax4(of[3], umax4(of[2], umax4(of[1], umax4(of[0], 0)))) == ~0u); ++it) { o_ld(tt); VMW(0); TIE2(of[0], of[1]); TIE2(of[2], of[3]); }
            STAMP(16 + tt * 2);
            f32x4 acc[4] = {{0, 0, 0, 0}, {0, 0, 0, 0}, {0, 0, 0, 0}, {0, 0, 0, 0}};
            sfor<0, 16>([&](auto ic) AI {
                CI(i, ic); constexpr int c = i >> 2, j = i & 3;
                acc[c] = mfma32(Wo[j][c], __builtin_bit_cast(bf16x8, of[j]), acc[c]);
                IF (c > 0 && j == 2) LD(f32x4, (tt & 1) * PWD + ((c - 1) >> 1) * 128 + (((c - 1) & 1) ? pa1 : pa0)) = acc[c - 1];
                IF (i & 1) side(IC<i / 2>{});
                SB();
            });
            LD(f32x4, (tt & 1) * PWD + 128 + pa1) = acc[3];
            SB();
        };
        auto o_ld1 = [&](int tt, auto jc) AI { CI(j, jc); ofb[tt & 1][j] = ld16c<j * 1024>(rw, l16, WS_O + ((rg * 4 + tt) * 16 + 4 * hd) * 1024); };
        w1_issue(1, 3, Z5);
        w1_read(1, Z1);
        VMW(7); wo_step(0, [&](auto ic) AI { CI(i, ic); IF (i < 6) w1_one(1, 4 + i / 3, i < 3 ? Z6 : Z7, IC<i % 3>{}); });
        XS(2);
        BAR();
        STAMP(17);
        w1_read(2, Z2); w1_read(3, Z3); w1_read(4, Z4);
        VMW(9); wo_step(1, [&](auto ic) AI { CI(i, ic); IF (i < 4) w2_one(hd, 0, 0, ic); else o_ld1(2, IC<i - 4>{}); });
        XS(3);
        s1_part(IC<0>{}, IC<2>{}, nof);
        VMW(8);
        XS(4);
        BAR();
        STAMP(19);
        s1_part(IC<2>{}, IC<5>{}, [&](auto ic) AI { w2_one(hd + 4, 0, 0, ic); });
        VMW(3); wo_step(2, [&](auto ic) AI { CI(i, ic); IF (i == 0) w2_one(hd + 4, 0, 0, IC<3>{}); IF (i >= 4) o_ld1(3, IC<i - 4>{}); });
        XS(6);
        BAR();
        STAMP(21);
        w1_read(5, Z8); w1_read(6, Z9);
        y_ld(0);
        s1_part(IC<5>{}, IC<7>{}, nof);
        XS(7);
        VMW(5); wo_step(3, [&](auto ic) AI { CI(i, ic); IF (i < 4) w2_one(hd + 4, 4, 64 * K, ic); });
        XS(8);
        BAR();
        STAMP(23);
        w1_read(7, Z3);
        VMW(4); y_put(0);
        STAMP(51);
        y_stat(0);
        y_ld(1);
        s1_part(IC<7>{}, IC<8>{}, nof);
#pragma unroll
        for (int m = 0; m < 3; ++m) LD(f32x4, L_S1 + (tk & 1) * 768 + (hd * 48 + m * 16 + g * 4) * 4) = sacc[m];
        XS(57);
    } else {
        unsigned prw = L_PW + pr; PIN(prw);
        f32x4 ga, A2;
        auto fin_wo = [&](int tt, auto side) AI {
            f32x4 pp[4];
#pragma unroll
            for (int ww = 0; ww < 4; ++ww) pp[ww] = LD(f32x4, prw + (tt & 1) * PWD + ww * 4096);
            if (tt == 0) {
                const u32x2 gv = LD(u32x2, L_GT + fb * 64 + fg * 16 + fe * 8);
                const u32x2 av2 = LD(u32x2, L_CB + 1024 + ((cg & 4) + ((cg - rot) & 3)) * 128 + fb * 64 + fg * 16 + fe * 8);
                ga = (f32x4){lo16(gv[0]), hi16(gv[0]), lo16(gv[1]), hi16(gv[1])};
                A2 = (f32x4){lo16(av2[0]), hi16(av2[0]), lo16(av2[1]), hi16(av2[1])};
            }
            side(IC<0>{});
            const u32x2 xr2 = xres[tt];
            const f32x4 xv = {lo16(xr2[0]), hi16(xr2[0]), lo16(xr2[1]), hi16(xr2[1])};
            const f32x4 sum = (pp[0] + pp[1]) + (pp[2] + pp[3]);
            const f32x4 v = ga * sum + xv;
            const f32x4 xx = v * A2;
            const unsigned y0 = pk2(xx[0], xx[1]), y1 = pk2(xx[2], xx[3]);
            const u32x4 yy = {y0, y1, dppu<0xF5>(y0), dppu<0xF5>(y1)};
            st16s(rw, fe ? OOB : ftok * 1024 + tk * 8, WS_Y + (rg * 4 + tt) * 16384 + cg * 128, yy);
            side(IC<1>{});
            const u32x2 x1 = {pk2(v[0], v[1]), pk2(v[2], v[3])};
            x1p[tt] = x1;
            float s = dot2(x1[1], 0x3f803f80u, dot2(x1[0], 0x3f803f80u, 0.f));
            const float qq = dot2(x1[1], x1[1], dot2(x1[0], x1[0], 0.f));
            asm volatile("s_nop 1\n\tv_add_f32_dpp %0, %0, %0 row_ror:8 row_mask:0xf bank_mask:0x3\n\tv_add_f32_dpp %0, %1, %1 row_ror:8 row_mask:0xf bank_mask:0xc\n\ts_nop 1" : "+v"(s) : "v"(qq));
            s += dpp<0x141>(s); s += dpp<0xB1>(s); s += dpp<0x4E>(s);
            st4s(rw, c8 ? OOB : ftok * 8 + fb * 4, WS_ST + ((rg * 4 + tt) * 8 + cg) * 128, F2U(s));
            side(IC<2>{});
            SB();
        };
        v1o_mk();
        VMW(3); if (PT2 >= 2) TIE1(pfd); TIE2(xres[0], xres[1]); TIE2(xres[2], xres[3]);
        XS(2);
        BAR();
        STAMP(17);
        fin_wo(0, [&](auto ic) AI { w1_one(0, 5, Z8, ic); });
        STAMP(18);
        s1_part(IC<0>{}, IC<3>{}, [&](auto ic) AI { w1_one(0, 6, Z9, ic); });
        XS(4);
        BAR();
        STAMP(19);
        fin_wo(1, [&](auto ic) AI { w1_one(0, 7, Z3, ic); });
        STAMP(20);
        w1_read(3, Z5); w1_one(1, 6, Z4, IC<0>{}); w1_read(4, Z6); w1_one(1, 6, Z4, IC<1>{}); w1_read(5, Z7); w1_one(1, 6, Z4, IC<2>{});
        XS(5);
        VMW(8);
        XS(6);
        BAR();
        STAMP(21);
        st4s(rw, lane == 0 ? 0u : OOB, WS_FL2 + rg * 128 + cg * 16 + hd * 4, tag);
        w1_issue(1, 7, Z5);
        fin_wo(2, nof);
        STAMP(22);
        s1_part(IC<3>{}, IC<6>{}, nof);
        XS(7);
        VMW(2);
        XS(8);
        BAR();
        STAMP(23);
        w1_read(6, Z4); w1_read(7, Z5);
        fin_wo(3, nof);
        STAMP(51);
        s1_part(IC<6>{}, IC<8>{}, nof);
        XS(57);
    }
    BAR();
    STAMP(24);

    h_rd(0, 0); h_rd(1, 0); h_rd(2, 0); h_rd(3, 0); h_rd(4, 0); h_rd(5, 0); h_rd(6, 0); h_rd(7, 0);
    unsigned xp1 = L_XC1 + hd * 3072 + l16; PIN(xp1);
    unsigned s1a = L_S1 + hd * 192 + g * 16;
    unsigned vus0 = (g >> 1) * 256 + tk * 16 + (g & 1) * 512;
    unsigned ssa = L_SST + tk * 8;
    if (B) { PIN(s1a); PIN(vus0); PIN(ssa); PIN(vos); }
    const unsigned ks0 = w * 5 + (w < 3 ? w : 3);
    u32x4 uf[3][6];
    auto u_ld1 = [&](int tt, auto jc) AI {
        CI(j, jc);
        const unsigned so = WS_U + ((rg * 4 + tt) * 43 + ks0) * 1024;
        IF (j < 4) uf[tt % 3][j] = ld16c<j * 1024>(rw, l16, so);
        IF (j == 4) uf[tt % 3][4] = ld16c<0>(rw, l16, so + 4096);
        IF (j == 5 && A) uf[tt % 3][5] = ld16c<0>(rw, l16, w < 3 ? so + 5120 : OOB);
    };
    auto u_ld = [&](int tt) AI {
        const unsigned so = WS_U + ((rg * 4 + tt) * 43 + ks0) * 1024;
        u32x4* u = uf[tt % 3];
        u[0] = ld16c<0>(rw, l16, so); u[1] = ld16c<1024>(rw, l16, so); u[2] = ld16c<2048>(rw, l16, so); u[3] = ld16c<3072>(rw, l16, so);
        u[4] = ld16c<0>(rw, l16, so + 4096);
        if (A) u[5] = ld16c<0>(rw, l16, w < 3 ? so + 5120 : OOB);
    };
    f32x4 acc[3], oa[3], pa[3], t1v[3], s1v[3], ev[3]; f32x2 st; unsigned ua[3], ub[3];
    auto silu = [&](auto tc, auto ic) AI {
        CI(tt, tc); CI(i, ic);
        const unsigned xb = xp1 + (tt & 1) * 12288;
        auto Lm = [&](int m) AI { s1v[m] = LD(f32x4, s1a + 768 + m * 64); t1v[m] = LD(f32x4, s1a + m * 64); };
        auto Pm = [&](int m) AI {
#pragma unroll
            for (int r = 0; r < 4; ++r) oa[m][r] = __builtin_fmaf(st.y, __builtin_fmaf(st.x, s1v[m][r], oa[m][r]), t1v[m][r]);
        };
        auto Zm = [&](int m) AI {
#pragma unroll
            for (int r = 0; r < 4; ++r) oa[m][r] = __builtin_fmaf(st.y, pa[m][r], oa[m][r]);
        };
        auto Em = [&](int m) AI {
#pragma unroll
            for (int r = 0; r < 4; ++r) ev[m][r] = __builtin_amdgcn_exp2f(oa[m][r]);
        };
        auto Cm = [&](int m) AI {
#pragma unroll
            for (int r = 0; r < 4; ++r) ev[m][r] = __builtin_amdgcn_rcpf(ev[m][r]);
        };
        auto Fm = [&](int m) AI { oa[m] *= ev[m]; ua[m] = pk2(oa[m][0], oa[m][1]); ub[m] = pk2(oa[m][2], oa[m][3]); };
        if (i == 0) { st = LD(f32x2, ssa + tt * 128); Lm(0); Lm(1); }
        if (i == 3) { Pm(0); }
        if (i == 4) { pa[0] = LD(f32x4, xb); pa[1] = LD(f32x4, xb + 1024); Lm(2); }
        if (i == 5) Pm(1);
        if (i == 6) pa[2] = LD(f32x4, xb + 2048);
        if (i == 7) Zm(0);
        if (i == 8) Em(0);
        if (i == 9) { Zm(1); ev[0] += 1.f; }
        if (i == 10) Cm(0);
        if (i == 11) Em(1);
        if (i == 12) { Fm(0); ev[1] += 1.f; }
        if (i == 13) Cm(1);
        if (i == 14) Pm(2);
        if (i == 15) Fm(1);
        const unsigned so = WS_U + (rg * 4 + tt) * 44032 + (tile0 + hd * 3) * 512;
        if (i == 16) {
            auto ra_ = __builtin_amdgcn_permlane16_swap(ua[0], ua[1], false, false);
            auto rb_ = __builtin_amdgcn_permlane16_swap(ub[0], ub[1], false, false);
            st16s(rw, vus0, (cg < 7 || hd > 0) ? so : OOB, (u32x4){ra_[0], rb_[0], ra_[1], rb_[1]});
            Zm(2);
        }
        if (i == 17) Em(2);
        if (i == 18) ev[2] += 1.f;
        if (i == 19) Cm(2);
        if (i == 20) Fm(2);
        if (i == 21) {
            auto ra_ = __builtin_amdgcn_permlane16_swap(ua[2], ua[2], false, false);
            auto rb_ = __builtin_amdgcn_permlane16_swap(ub[2], ub[2], false, false);
            st16s(rw, vos, hd < 3 ? so + 1024 : OOB, (u32x4){ra_[0], rb_[0], ra_[1], rb_[1]});
        }
    };
    auto fside = [&](auto tc, auto ic) AI {
        CI(tt, tc); CI(i, ic);
        constexpr unsigned AN = ((tt + 1) & 1) * 16384;
        if (tt > 0 && i == 4) { STAMP(57 + tt); BAR(); }
        if (tt == 0 && i == 2) row_read(W2[0], 0);
        if (tt == 1 && i == 22) row_read(W2[1], 32 * K);
        if (B) {
            IF (tt > 0) silu(IC<tt - 1>{}, ic);
            if (tt == 0 && i == 6) {
                const float sc1 = (tk & 1) ? 1.f : -1.44269504089f;
#pragma unroll
                for (int m = 0; m < 3; ++m) {
                    const unsigned o = L_S1 + (tk & 1) * 768 + (hd * 48 + m * 16 + g * 4) * 4;
                    LD(f32x4, o) = (LD(f32x4, o) + sacc[m]) * sc1;
                }
            }
        } else {
            IF (tt > 0 && i < 3) LD(f32x4, xp1 + ((tt - 1) & 1) * 12288 + i * 1024) = oa[i];
            IF (tt == 0 && i < 16 && !(i & 1)) w2_one(i < 8 ? hd : hd + 4, 1, 32 * K, IC<(i / 2) & 3>{});
            IF (tt == 1 && i < 16 && !(i & 1)) w2_one(i < 8 ? hd : hd + 4, 2, 0, IC<(i / 2) & 3>{});
            IF (tt == 2 && i < 8 && !(i & 1)) w2_one(hd, 5, 64 * K, IC<(i / 2) & 3>{});
            IF (tt == 2 && i >= 8 && i < 16 && !(i & 1)) w2_one(hd + 4, 3, 32 * K, IC<(i / 2) & 3>{});
            IF (tt == 3 && i < 16 && !(i & 1)) w2_one(hd, i < 8 ? 4 : 3, i < 8 ? 32 * K : 0, IC<(i / 2) & 3>{});
            if (tt == 0 && i == 5) { VMW(3); y_put(1); }
            if (tt == 0 && i == 7) { y_stat(1); y_ld(2); }
            if (tt == 0 && i == 21) { VMW(4); y_put(2); }
            if (tt == 0 && i == 23) { y_stat(2); y_ld(3); }
            if (tt == 1 && i == 3) VMW(7);
            if (tt == 1 && i == 19) { VMW(8); y_put(3); }
            if (tt == 1 && i == 21) y_stat(3);
            if (tt == 1 && i == 23) flg = ld4c(rw, (lane & 31) * 4, WS_FL2 + rg * 128);
            if (tt == 2 && i == 21) { VMW(8); row_read(W2[2], 0); }
            if (tt == 2 && i == 23) {
                TIE1(flg);
#pragma nounroll
                for (int it = 0; it < SPIN && anyl(flg != tag); ++it) { flg = ld4c(rw, (lane & 31) * 4, WS_FL2 + rg * 128); VMW(0); TIE1(flg); }
            }
        }
        if (tt == 0 && i == 8) { STAMP(52); BAR(); h_rd(0, AN); h_rd(1, AN); }
        if (tt > 0 && tt < 3 && i == 5) h_rd(0, AN);
        if (tt < 3 && i >= (tt ? 5 : 8) && i % 3 == 2) h_rd(i / 3, AN);
        if (tt == 3 && i == (A ? 20 : 22)) u_ld(0);
        SB();
    };
    sfor<0, 4>([&](auto tc) AI {
        CI(tt, tc);
#pragma unroll
        for (int m = 0; m < 3; ++m) { oa[m] = acc[m]; acc[m] = (f32x4){0, 0, 0, 0}; }
        SB();
        sfor<0, 24>([&](auto ic) AI { CI(i, ic); acc[i % 3] = mfma32(W1[i / 3][i % 3], hf[i / 3], acc[i % 3]); fside(tc, ic); });
        STAMP(25 + tt);
    });

    if (A) {
#pragma unroll
        for (int m = 0; m < 3; ++m) LD(f32x4, xp1 + 12288 + m * 1024) = acc[m];
        VMW(14);
        BAR();
    } else {
#pragma unroll
        for (int m = 0; m < 3; ++m) oa[m] = acc[m];
        sfor<0, 4>([&](auto ic) AI { silu(IC<3>{}, ic); });
        BAR();
        silu(IC<3>{}, IC<4>{});
    }
    STAMP(29);
    RELANE();
    u_ld(1);
    if (A) { VMW(12); row_read(W2[3], 0); row_read(W2[4], 32 * K); unsigned tb = 64 * K; row_read(W2[5], tb); }
    else { row_read(W2[2], 0); silu(IC<3>{}, IC<5>{}); silu(IC<3>{}, IC<6>{}); row_read(W2[3], 32 * K); silu(IC<3>{}, IC<7>{}); silu(IC<3>{}, IC<8>{}); row_read(W2[4], 64 * K); }
    f32x4 gb;
    if (B) {
        const u32x2 gv = LD(u32x2, L_GT + 128 + fb * 64 + fg * 16 + fe * 8);
        gb = (f32x4){lo16(gv[0]), hi16(gv[0]), lo16(gv[1]), hi16(gv[1])} * (-1.f / 1.44269504089f);
    }
    unsigned pw1 = pw0 ^ 64; PIN(pw0); PIN(pw1); if (B) PIN(pr);
    f32x4 pp[8];
    auto fin_out = [&](auto tc, auto ic) AI {
        CI(tt, tc); CI(i, ic);
        IF (i < 2) {
#pragma unroll
            for (int ww = 4 * i; ww < 4 * i + 4; ++ww) pp[ww] = LD(f32x4, pr + (tt & 1) * 32768 + ww * 4096);
        }
        if (i == 5) pp[0] += pp[1];
        if (i == 6) pp[2] += pp[3];
        if (i == 7) pp[4] += pp[5];
        if (i == 8) pp[6] += pp[7];
        if (i == 9) pp[0] += pp[2];
        if (i == 10) pp[4] += pp[6];
        const u32x2 xp2 = x1p[tt];
        if (i == 11) { pp[0] += pp[4]; }
        if (i == 12) pp[1] = (f32x4){lo16(xp2[0]), hi16(xp2[0]), lo16(xp2[1]), hi16(xp2[1])};
        if (i == 13) pp[0] = gb * pp[0] + pp[1];
        if (i == 14) __builtin_amdgcn_raw_buffer_store_b64((u32x2){pk2(pp[0][0], pp[0][1]), pk2(pp[0][2], pp[0][3])}, rout, (int)fvo, (int)((rg * 4 + tt) * 16384 + cg * 128), 0);
    };
    constexpr int NU = A ? 6 : 5;
    unsigned um;
    auto u_chk = [&](int tt, auto jc) AI {
        CI(j, jc);
        IF (j < NU) { TIE1(uf[tt % 3][j]); um = umax4(uf[tt % 3][j], j ? um : 0u); }
    };
    sfor<0, 4>([&](auto tc) AI {
        CI(tt, tc);
        u32x4* u = uf[tt % 3];
        IF (B && tt > 0) { fin_out(IC<tt - 1>{}, IC<0>{}); fin_out(IC<tt - 1>{}, IC<1>{}); }
        IF (tt == 0 || tt == 3) {
            VMW(tt == 0 ? NU : 0);
            sfor<0, 6>([&](auto jc) AI { u_chk(tt, jc); });
        }
#pragma nounroll
        for (int it = 0; it < SPIN && anyl(um == ~0u); ++it) {
            u_ld(tt); VMW(0);
            sfor<0, 6>([&](auto jc) AI { u_chk(tt, jc); });
        }
        STAMP(30 + tt * 2);
        f32x4 a2[4] = {{0, 0, 0, 0}, {0, 0, 0, 0}, {0, 0, 0, 0}, {0, 0, 0, 0}};
        auto pwr = [&](int c) AI { LD(f32x4, (tt & 1) * 32768 + (c >> 1) * 128 + ((c & 1) ? pw1 : pw0)) = a2[c]; };
        if (A) {
            if (w < 3) {
#pragma unroll
                for (int c = 0; c < 4; ++c) a2[c] = mfma32(W2[5][c], __builtin_bit_cast(bf16x8, u[5]), a2[c]);
            }
            SB();
            sfor<0, 20>([&](auto ic) AI {
                CI(i, ic); constexpr int c = i & 3, s = i >> 2;
                a2[c] = mfma32(W2[s][c], __builtin_bit_cast(bf16x8, u[s]), a2[c]);
                IF ((tt == 0 || tt == 2) && i % 3 == 0 && i < 18) u_ld1(tt ? 3 : 2, IC<i / 3>{});
                SB();
            });
            pwr(0); pwr(1); pwr(2); pwr(3);
            IF (tt < 2) { VMW(tt ? 0 : 6); sfor<0, 6>([&](auto jc) AI { u_chk(tt + 1, jc); }); }
        } else {
            IF (tt == 0) sfor<9, 22>([&](auto ic) AI { silu(IC<3>{}, ic); });
            IF (tt == 2) { u_ld1(3, IC<0>{}); u_ld1(3, IC<1>{}); }
            IF (tt > 0) sfor<2, 15>([&](auto ic) AI { fin_out(IC<tt - 1>{}, ic); });
            IF (tt < 2) { VMW(tt ? 1 : 2); sfor<0, 6>([&](auto jc) AI { u_chk(tt + 1, jc); }); }
            SB();
            sfor<0, 20>([&](auto ic) AI {
                CI(i, ic); constexpr int c = (i / 10) * 2 + (i & 1), s = (i % 10) >> 1;
                a2[c] = mfma32(W2[s][c], __builtin_bit_cast(bf16x8, u[s]), a2[c]);
                IF (tt == 0 && i % 3 == 0 && i < 15) u_ld1(2, IC<i / 3>{});
                IF (tt == 2 && i % 3 == 0 && i < 9) u_ld1(3, IC<2 + i / 3>{});
                IF (i == 13) pwr(0);
                IF (i == 16) pwr(1);
                SB();
            });
            pwr(2); pwr(3);
        }
        BAR();
        STAMP(31 + tt * 2);
    });
    if (B) {
        sfor<0, 15>([&](auto ic) AI { fin_out(IC<3>{}, ic); });
    }
    STAMP(38); STAMPC(63);
    IF (B) asm volatile(".Lpfe:");
    IF (A) asm volatile(".Lpfa:");
    __builtin_amdgcn_endpgm();
    };
    if (kh == 0) body(IC<0>{});
    body(IC<1>{});
}

#if defined(__HIP_DEVICE_COMPILE__) && !defined(PK)
#pragma clang attribute pop
#endif
extern "C" void kernel_launch(void* const* d_in, const int* in_sizes, int n_in, void* d_out, int out_size, void* d_ws, size_t ws_size, hipStream_t stream) {
    fused_block<<<256, 512, 0, stream>>>((const char*)d_in[0], (const char*)d_in[1], (const char*)d_in[2], (const char*)d_in[4], (const char*)d_in[5],
                                         (const char*)d_in[6], (const char*)d_in[7], (char*)d_ws, (char*)d_out);
}
```

```cpp
#include <hip/hip_runtime.h>
#include <stdint.h>

#if defined(__HIP_DEVICE_COMPILE__) && !defined(PK)
#pragma clang attribute push(__attribute__((target("no-packed-fp32-ops"))), apply_to = function)
#endif
#define LAS __attribute__((address_space(3)))
typedef __bf16 bf16x8 __attribute__((ext_vector_type(8)));
typedef __bf16 bf16x4 __attribute__((ext_vector_type(4)));
typedef __bf16 bf16x2 __attribute__((ext_vector_type(2)));
typedef short s16x4 __attribute__((ext_vector_type(4)));
typedef float f32x4 __attribute__((ext_vector_type(4)));
typedef float f32x2 __attribute__((ext_vector_type(2)));
typedef unsigned u32x4 __attribute__((ext_vector_type(4)));
typedef unsigned u32x2 __attribute__((ext_vector_type(2)));
typedef __amdgpu_buffer_rsrc_t rsrc_t;
#define DI __device__ __forceinline__
#define U2F(x) __builtin_bit_cast(float, (unsigned)(x))
#define F2U(x) __builtin_bit_cast(unsigned, (float)(x))
#define EXP 0
#define PF 13
#define OUTF 0
#define PT1 0
#define PT2 3
#define MAP 1
#define ROT 1

constexpr unsigned WS_EPOCH = 0, WS_FLAG = 4096, WS_MODP = 8192, WS_ST = 65536, WS_O = 1u << 20, WS_Y = 3u << 20, WS_U = 5u << 20, WS_BYTES = 11u << 20;
constexpr int SPIN = 1 << 15;
constexpr unsigned K = 1024;
constexpr unsigned L_M = 0, L_STAT = 144 * K, L_RST = 149 * K, L_SC = 150 * K, L_AS = 152 * K, L_MK = 156 * K;
constexpr unsigned L_H = 121 * K, L_X = 97 * K;
constexpr unsigned L_CB = 153 * K, L_GT = 155 * K, L_SST = 155 * K + 512, L_S1 = 156 * K;
constexpr unsigned L_PW = 121 * K, PWD = 16 * K, L_ACT = 97 * K, L_XC1 = 129 * K;
constexpr unsigned Z0 = 0, Z1 = 11 * K, Z2 = 22 * K, Z3 = 33 * K, Z4 = 44 * K, Z5 = 64 * K, Z6 = 75 * K, Z7 = 86 * K, Z8 = 97 * K, Z9 = 108 * K;

DI unsigned pk2(float a, float b) { bf16x2 v = {(__bf16)a, (__bf16)b}; return __builtin_bit_cast(unsigned, v); }
DI float lo16(unsigned u) { return U2F(u << 16); }
DI float hi16(unsigned u) { return U2F(u & 0xffff0000u); }
template <int CTRL> DI float dpp(float v) { return U2F(__builtin_amdgcn_update_dpp(0, (int)F2U(v), CTRL, 0xf, 0xf, true)); }
template <int CTRL> DI unsigned dppu(unsigned v) { return (unsigned)__builtin_amdgcn_update_dpp(0, (int)v, CTRL, 0xf, 0xf, true); }
DI float fmx(float a, float b) { return __builtin_elementwise_maximum(a, b); }
DI float dot2(unsigned a, unsigned b, float c) { return __builtin_amdgcn_fdot2_f32_bf16(__builtin_bit_cast(bf16x2, a), __builtin_bit_cast(bf16x2, b), c, false); }
DI float sw16add(float a, float b) { auto r = __builtin_amdgcn_permlane16_swap(F2U(a), F2U(b), false, false); return U2F(r[0]) + U2F(r[1]); }
DI float sw32add(float a, float b) { auto r = __builtin_amdgcn_permlane32_swap(F2U(a), F2U(b), false, false); return U2F(r[0]) + U2F(r[1]); }
DI float xmax16(float v) { auto r = __builtin_amdgcn_permlane16_swap(F2U(v), F2U(v), false, false); return fmx(U2F(r[0]), U2F(r[1])); }
DI float xmax32(float v) { auto r = __builtin_amdgcn_permlane32_swap(F2U(v), F2U(v), false, false); return fmx(U2F(r[0]), U2F(r[1])); }
DI f32x4 mfma32(bf16x8 a, bf16x8 b, f32x4 c) { return __builtin_amdgcn_mfma_f32_16x16x32_bf16(a, b, c, 0, 0, 0); }
DI f32x4 mfma32u(u32x4 a, u32x4 b, f32x4 c) { return mfma32(__builtin_bit_cast(bf16x8, a), __builtin_bit_cast(bf16x8, b), c); }
DI f32x4 mfma16u(unsigned a0, unsigned a1, unsigned b0, unsigned b1, f32x4 c) {
    return __builtin_amdgcn_mfma_f32_16x16x16bf16_1k(__builtin_bit_cast(s16x4, (u32x2){a0, a1}), __builtin_bit_cast(s16x4, (u32x2){b0, b1}), c, 0, 0, 0);
}
DI rsrc_t mk(const void* p, unsigned n) { return __builtin_amdgcn_make_buffer_rsrc((void*)p, (short)0, (int)n, 0x00020000); }
DI unsigned nosent(unsigned v) { return v < 0xfffffffeu ? v : 0xfffffffeu; }
DI unsigned umax3(unsigned a, unsigned b, unsigned c) { unsigned m = a > b ? a : b; return m > c ? m : c; }
DI unsigned umax4(u32x4 v, unsigned m) { return umax3(umax3(v[0], v[1], m), v[2], v[3]); }

#define BAR() asm volatile("s_waitcnt lgkmcnt(0)\n\ts_barrier" ::: "memory")
#define VMW(n) asm volatile("s_waitcnt vmcnt(%0)" ::"n"(n) : "memory")
#define TIE1(a) asm volatile("; TIE %0" : "+v"(a))
#define TIE2(a, b) asm volatile("; TIE %0 %1" : "+v"(a), "+v"(b))
DI void dma1(rsrc_t r, unsigned m0, unsigned vo, unsigned so) {
    asm volatile("s_mov_b32 m0, %0\n\ts_nop 0\n\tbuffer_load_dwordx4 %1, %2, %3 offen lds" ::"s"(m0), "v"(vo), "s"(r), "s"(so) : "m0", "memory");
}
template <int OFF> DI void dma1o(rsrc_t r, unsigned m0, unsigned vo, unsigned so) {
    asm volatile("s_mov_b32 m0, %0\n\ts_nop 0\n\tbuffer_load_dwordx4 %1, %2, %3 offen offset:%4 lds" ::"s"(m0), "v"(vo), "s"(r), "s"(so), "n"(OFF) : "m0", "memory");
}
DI void touch(unsigned& d, rsrc_t r, unsigned vo, unsigned so) {
    asm volatile("buffer_load_dword %0, %1, %2, %3 offen ; touch" : "+v"(d) : "v"(vo), "s"(r), "s"(so) : "memory");
}
DI void dma3(rsrc_t r, unsigned m0, unsigned vo, unsigned so) {
    asm volatile("s_mov_b32 m0, %0\n\ts_nop 0\n\tbuffer_load_dwordx4 %1, %2, %3 offen lds\n\tbuffer_load_dwordx4 %1, %2, %3 offen offset:1024 lds\n\tbuffer_load_dwordx4 %1, %2, %3 offen offset:2048 lds"
                 ::"s"(m0), "v"(vo), "s"(r), "s"(so) : "m0", "memory");
}
DI void dma3v(rsrc_t r, unsigned m0, unsigned v0, unsigned v1, unsigned v2, unsigned so) {
    asm volatile("s_mov_b32 m0, %0\n\ts_nop 0\n\tbuffer_load_dwordx4 %1, %4, %5 offen lds\n\tbuffer_load_dwordx4 %2, %4, %5 offen offset:1024 lds\n\tbuffer_load_dwordx4 %3, %4, %5 offen offset:2048 lds"
                 ::"s"(m0), "v"(v0), "v"(v1), "v"(v2), "s"(r), "s"(so) : "m0", "memory");
}
DI void dma4(rsrc_t r, unsigned m0, unsigned vo0, unsigned vo1, unsigned so) {
    asm volatile("s_mov_b32 m0, %0\n\ts_nop 0\n\tbuffer_load_dwordx4 %1, %3, %4 offen lds\n\tbuffer_load_dwordx4 %2, %3, %5 offen offset:1024 lds\n\t"
                 "buffer_load_dwordx4 %1, %3, %6 offen offset:2048 lds\n\tbuffer_load_dwordx4 %2, %3, %7 offen offset:3072 lds"
                 ::"s"(m0), "v"(vo0), "v"(vo1), "s"(r), "s"(so), "s"(so + 7168u), "s"(so + 14336u), "s"(so + 21504u) : "m0", "memory");
}
template <int OFF> DI u32x4 ld16(rsrc_t r, unsigned vo, unsigned so) {
    u32x4 v; asm volatile("buffer_load_dwordx4 %0, %1, %2, %3 offen offset:%4" : "=v"(v) : "v"(vo), "s"(r), "s"(so), "n"(OFF) : "memory"); return v;
}
template <int OFF> DI u32x4 ld16c(rsrc_t r, unsigned vo, unsigned so) {
    u32x4 v; asm volatile("buffer_load_dwordx4 %0, %1, %2, %3 offen offset:%4 sc1" : "=v"(v) : "v"(vo), "s"(r), "s"(so), "n"(OFF) : "memory"); return v;
}
DI u32x2 ld8(rsrc_t r, unsigned vo, unsigned so) {
    u32x2 v; asm volatile("buffer_load_dwordx2 %0, %1, %2, %3 offen" : "=v"(v) : "v"(vo), "s"(r), "s"(so) : "memory"); return v;
}
DI unsigned ld4(rsrc_t r, unsigned vo, unsigned so) {
    unsigned v; asm volatile("buffer_load_dword %0, %1, %2, %3 offen" : "=v"(v) : "v"(vo), "s"(r), "s"(so) : "memory"); return v;
}
DI unsigned ld4c(rsrc_t r, unsigned vo, unsigned so) {
    unsigned v; asm volatile("buffer_load_dword %0, %1, %2, %3 offen sc1" : "=v"(v) : "v"(vo), "s"(r), "s"(so) : "memory"); return v;
}
DI void st16c(rsrc_t r, unsigned vo, u32x4 v) { __builtin_amdgcn_raw_buffer_store_b128(v, r, (int)vo, 0, 16); }
DI void st4c(rsrc_t r, unsigned vo, unsigned v) { __builtin_amdgcn_raw_buffer_store_b32(v, r, (int)vo, 0, 16); }
DI void st16s(rsrc_t r, unsigned vo, unsigned so, u32x4 v) { __builtin_amdgcn_raw_buffer_store_b128(v, r, (int)vo, (int)so, 16); asm volatile("s_nop 1" ::"v"(v) : "memory"); }
DI void st16n(rsrc_t r, unsigned vo, unsigned so, u32x4 v) { __builtin_amdgcn_raw_buffer_store_b128(v, r, (int)vo, (int)so, 16); }
DI void st4s(rsrc_t r, unsigned vo, unsigned so, unsigned v) { __builtin_amdgcn_raw_buffer_store_b32(v, r, (int)vo, (int)so, 16); }
#define SB() __builtin_amdgcn_sched_barrier(0)
#define PIN(a) asm volatile("" : "+v"(a))
#define UNLIKELY(c) __builtin_expect(!!(c), 0)
constexpr unsigned OOB = 0x40000000u;
DI unsigned newlane() { unsigned l; asm volatile("v_mbcnt_lo_u32_b32 %0, -1, 0\n\tv_mbcnt_hi_u32_b32 %0, -1, %0" : "=v"(l)); return l; }
DI bool anyl(bool c) { return __builtin_amdgcn_ballot_w64(c) != 0; }

#define STAMP(i)
#define STAMPC(i)

#define XS(i)
template <int N> struct IC { static constexpr int v = N; };
template <int I, int N, class F> DI void sfor_(F& f) { if constexpr (I < N) { f(IC<I>{}); sfor_<I + 1, N>(f); } }
template <int I, int N, class F> DI void sfor(F f) { sfor_<I, N>(f); }
#define AI __attribute__((always_inline))
#define IF(c) if constexpr (c)
#define CI(n, c) constexpr int n = decltype(c)::v
DI void dma4s(rsrc_t r, unsigned m0, unsigned vo, unsigned so, unsigned stride) {
    asm volatile("s_mov_b32 m0, %0\n\ts_nop 0\n\tbuffer_load_dwordx4 %1, %2, %3 offen lds\n\tbuffer_load_dwordx4 %1, %2, %4 offen offset:1024 lds\n\t"
                 "buffer_load_dwordx4 %1, %2, %5 offen offset:2048 lds\n\tbuffer_load_dwordx4 %1, %2, %6 offen offset:3072 lds"
                 ::"s"(m0), "v"(vo), "s"(r), "s"(so), "s"(so + stride - 1024u), "s"(so + 2 * (stride - 1024u)), "s"(so + 3 * (stride - 1024u)) : "m0", "memory");
}

__global__ __launch_bounds__(512) void fused_block(const char* x, const char* wqkv, const char* wo, const char* w1, const char* w2,
                                                   const char* cvec, const char* wmod, char* ws, char* out) {
    __shared__ __attribute__((aligned(16))) char lds[163840];
    LAS char* L = (LAS char*)lds;
    const unsigned LB = (unsigned)(uintptr_t)L;
#define LD(T, off) (*(LAS T*)(L + (off)))
#define TRF(off, d) __builtin_shufflevector(__builtin_amdgcn_ds_read_tr16_b64_v4bf16((LAS bf16x4*)(L + (off))), __builtin_amdgcn_ds_read_tr16_b64_v4bf16((LAS bf16x4*)(L + (off) + (d))), 0, 1, 2, 3, 4, 5, 6, 7)
    unsigned lane = __builtin_amdgcn_workitem_id_x() & 63; const unsigned w = __builtin_amdgcn_readfirstlane(__builtin_amdgcn_workitem_id_x() >> 6);
    const unsigned b = __builtin_amdgcn_workgroup_id_x();
    STAMP(0); STAMPC(62);
    const unsigned rg = ((b & 7) >> 1) * 8 + (b >> 5), cg = (b & 1) * 4 + ((b >> 3) & 3);
    const unsigned rot = rg & 3;
    unsigned r8, c8, tk, g, q, p, l16, vy, swz0, fl, vo1k0, vo1k1, tre, tro, vos;
    auto derive = [&]() AI {
        r8 = lane >> 3; c8 = lane & 7; tk = lane & 15; g = lane >> 4; q = (lane & 15) >> 2; p = lane & 3;
        l16 = lane * 16; vy = r8 * 1024 + c8 * 16; swz0 = (c8 ^ (r8 & 2)) * 16; fl = ((q >> 1) & 1) | ((g & 1) << 1);
        vo1k0 = r8 * 1024 + swz0; vo1k1 = vo1k0 ^ 64;
        const unsigned trb = (8 * g + q) * 128 + (p >> 1) * 16 + (p & 1) * 8 + w * 4096;
        tre = trb + (fl << 1) * 16; tro = trb + (2 ^ (fl << 1)) * 16;
        vos = (g & 1) ? OOB : (g >> 1) * 256 + tk * 16;
    };
    derive();
#define RELANE() do { lane = newlane(); derive(); } while (0)
    const unsigned hd = w & 3, kh = w >> 2;
    const rsrc_t rx = mk(x, 2048 * 1024), rq = mk(wqkv, 512 * 3072), ro = mk(wo, 512 * 1024), r1 = mk(w1, 512 * 2752), r2 = mk(w2, 1376 * 1024);
    const rsrc_t rc = mk(cvec, 1024), rm = mk(wmod, 512 * 6144), rw = mk(ws, WS_BYTES), rout = mk(out, 2048 * 1024);
    const bool prod = b < 192 && w < 4;

    unsigned pfd = 0;
    {
        uint64_t pc; unsigned len;
        asm volatile("s_getpc_b64 %0\n.Lpfb:\n\ts_mov_b32 %1, .Lpfe-.Lpfb\n\ts_max_u32 %1, %1, .Lpfa-.Lpfb" : "=s"(pc), "=s"(len) :: "scc");
        touch(pfd, mk((const void*)pc, len), lane < 4 ? lane * 64 : OOB, ((b >> 3) * 8 + w) * 256);
    }
    unsigned ep = ld4c(rw, 0, WS_EPOCH);
    unsigned cv;
    if (prod) {
        cv = ld4(rc, lane * 4, (b & 3) * 256);
        dma4s(rm, LB + L_M + w * 4096, (lane >> 1) * 6144 + (lane & 1) * 16, (b & 3) * (128 * 6144) + (b >> 2) * 128 + w * 32, 32 * 6144);
    }
    u32x4 xr[10];
#pragma unroll
    for (int n = 0; n < 10; ++n) {
        int row = (int)rg * 64 - 16 + n * 8; if (row < 0) row += 16;
        xr[n] = ld16<0>(rx, vy, (unsigned)row * 1024 + w * 128);
    }
    constexpr int NT = (PF & 4) ? 1 : 0;
    {
        const char* tp = w == 0 ? wo : w == 1 ? w1 : w == 2 ? w2 : w == 3 ? out + rg * 65536 : w == 4 ? ws + WS_O + rg * 65536 : w == 5 ? ws + WS_Y + rg * 65536 : w == 6 ? ws + WS_U + rg * 176128 : w2 + 1375 * 1024;
        touch(pfd, mk(tp, 4), lane == 0 ? 0u : OOB, 0);
    }
    const unsigned vq = r8 * 3072 + (swz0 ^ ((w & 1) * 64));
    auto qkv_issue = [&](int r, unsigned slot) AI {
        const unsigned ks = kh * 8 + ((r + 2 * rot) & 7);
        dma3(rq, LB + slot + kh * 12288 + hd * 3072, vq, (ks * 32 + hd * 8) * 3072 + cg * 128);
    };
    STAMP(1);
    if (prod) {
        VMW(14 + NT); TIE2(ep, cv);
        const float a = lo16(cv), c = hi16(cv);
        LD(unsigned, L_SC + w * 256 + lane * 4) = pk2(a * __builtin_amdgcn_rcpf(1.f + __builtin_amdgcn_exp2f(-1.44269504089f * a)), c * __builtin_amdgcn_rcpf(1.f + __builtin_amdgcn_exp2f(-1.44269504089f * c)));
        VMW(10 + NT);
        f32x4 acc = {0, 0, 0, 0};
        const unsigned a0 = L_M + w * 4096 + (8 * g + q) * 32 + p * 8;
#pragma unroll
        for (int s = 0; s < 4; ++s) acc = mfma32(TRF(a0 + s * 1024, 128), LD(bf16x8, L_SC + w * 256 + s * 64 + g * 16), acc);
        u32x4 v;
#pragma unroll
        for (int j = 0; j < 4; ++j) v[j] = __builtin_amdgcn_perm(F2U(acc[j]), ep + 0x01010101u, 0x07060500u | j);
        st16s(rw, tk == 0 ? g * 16 : OOB, WS_MODP + (b * 64 + w * 16) * 4, v);
    } else if (w == 4) {
        const float NI = -__builtin_inff();
        f32x4 mcur, mprev; bool vc[4], vp[4];
#pragma unroll
        for (int r = 0; r < 4; ++r) {
            const int d = (int)tk - (int)(4 * g + r); vc[r] = d >= 0 && !(d & 1); vp[r] = d <= -2 && !(d & 1);
            mcur[r] = vc[r] ? 0.f : NI; mprev[r] = vp[r] ? 0.f : NI;
        }
        const u32x4 M = {(vp[0] ? 0xffffu : 0u) | (vp[1] ? 0xffff0000u : 0u), (vp[2] ? 0xffffu : 0u) | (vp[3] ? 0xffff0000u : 0u),
                         (vc[0] ? 0xffffu : 0u) | (vc[1] ? 0xffff0000u : 0u), (vc[2] ? 0xffffu : 0u) | (vc[3] ? 0xffff0000u : 0u)};
        LD(f32x4, L_MK + l16) = mcur; LD(f32x4, L_MK + 1024 + l16) = mprev; LD(u32x4, L_MK + 2048 + l16) = M;
        LD(u32x4, L_MK + 3072 + l16) = (u32x4){0x3f803f80u, 0x3f803f80u, 0x3f803f80u, 0x3f803f80u};
    }
    STAMP(2);
    qkv_issue(0, 48 * K); qkv_issue(1, 72 * K);
    float t[10][8];
#pragma unroll
    for (int h5 = 0; h5 < 2; ++h5) {
        if (h5 == 0) { VMW(11 + NT); } else { VMW(9 + NT); }
        float s[5], qq[5];
#pragma unroll
        for (int n = 0; n < 5; ++n) {
            TIE1(xr[h5 * 5 + n]);
            const u32x4 v = xr[h5 * 5 + n];
            s[n] = dot2(v[3], 0x3f803f80u, dot2(v[2], 0x3f803f80u, dot2(v[1], 0x3f803f80u, dot2(v[0], 0x3f803f80u, 0.f))));
            qq[n] = dot2(v[3], v[3], dot2(v[2], v[2], dot2(v[1], v[1], dot2(v[0], v[0], 0.f))));
        }
#define A5(F) F(0) F(1) F(2) F(3) F(4)
#define ST_A(n) "v_add_f32_dpp %" #n ", %" #n ", %" #n " row_half_mirror row_mask:0xf bank_mask:0x5\n\t"
#define ST_C(n) "v_add_f32_dpp %" #n ", %" #n ", %" #n " quad_perm:[1,0,3,2] row_mask:0xf bank_mask:0xf\n\t"
#define ST_D(n) "v_add_f32_dpp %" #n ", %" #n ", %" #n " quad_perm:[2,3,0,1] row_mask:0xf bank_mask:0xf\n\t"
#define ST_B(n, m) "v_add_f32_dpp %" #n ", %" #m ", %" #m " row_half_mirror row_mask:0xf bank_mask:0xa\n\t"
        asm volatile("s_nop 1\n\t" A5(ST_A) ST_B(0, 5) ST_B(1, 6) ST_B(2, 7) ST_B(3, 8) ST_B(4, 9) A5(ST_C) A5(ST_D)
                     : "+v"(s[0]), "+v"(s[1]), "+v"(s[2]), "+v"(s[3]), "+v"(s[4]) : "v"(qq[0]), "v"(qq[1]), "v"(qq[2]), "v"(qq[3]), "v"(qq[4]));
        unsigned sa = L_STAT + r8 * 64 + w * 8 + (c8 >> 2) * 4; PIN(sa);
#pragma unroll
        for (int n = 0; n < 5; ++n) LD(float, sa + (h5 * 5 + n) * 512) = s[n];
        if (h5 == 0) qkv_issue(2, 120 * K); else qkv_issue(3, 96 * K);
    }
    TIE1(ep);
    const unsigned tag = ep + 0x01010101u;
    auto mod_ld = [&](unsigned g0, unsigned g1, u32x4& a, u32x4& c) AI {
        a = ld16c<0>(rw, l16, WS_MODP + g0 * 1024); c = ld16c<0>(rw, l16, WS_MODP + g1 * 1024);
    };
    auto mod_bad = [&](u32x4 a, u32x4 c) AI {
        const unsigned ta = __builtin_amdgcn_perm(__builtin_amdgcn_perm(a[3], a[2], 0x0c0c0400u), __builtin_amdgcn_perm(a[1], a[0], 0x0c0c0400u), 0x05040100u);
        const unsigned tc = __builtin_amdgcn_perm(__builtin_amdgcn_perm(c[3], c[2], 0x0c0c0400u), __builtin_amdgcn_perm(c[1], c[0], 0x0c0c0400u), 0x05040100u);
        return anyl(ta != tag || tc != tag);
    };
    auto mod_fin = [&](unsigned g0, unsigned g1, u32x4 a, u32x4 c, float add1) AI {
        TIE2(a, c);
#pragma nounroll
        for (int it = 0; it < SPIN && mod_bad(a, c); ++it) { mod_ld(g0, g1, a, c); VMW(0); TIE2(a, c); }
        float s[4];
#pragma unroll
        for (int j = 0; j < 4; ++j) s[j] = sw16add(U2F(a[j] & 0xffffff00u), U2F(c[j] & 0xffffff00u));
        const unsigned r = pk2(sw32add(s[0], s[2]), sw32add(s[1], s[3]));
        const float ad = (g & 1) ? add1 : 0.f;
        return pk2(lo16(r) + ad, hi16(r) + ad);
    };
    auto unpack = [&](int n0, int n1) AI {
#pragma unroll
        for (int n = n0; n < n1; ++n)
#pragma unroll
            for (int j = 0; j < 4; ++j) { t[n][2 * j] = lo16(xr[n][j]); t[n][2 * j + 1] = hi16(xr[n][j]); PIN(t[n][2 * j]); PIN(t[n][2 * j + 1]); }
    };
    STAMP(3);
    BAR();
    STAMP(4);
    if (w == 4 || w == 5) {
        const unsigned row = w == 4 ? lane : 64 + tk;
        const f32x4 a = LD(f32x4, L_STAT + row * 64) + LD(f32x4, L_STAT + row * 64 + 16) + LD(f32x4, L_STAT + row * 64 + 32) + LD(f32x4, L_STAT + row * 64 + 48);
        const float mu = (a[0] + a[2]) * (1.f / 512.f);
        const float var = __builtin_fmaxf((a[1] + a[3]) * (1.f / 512.f) - mu * mu, 0.f);
        const float rs = __builtin_amdgcn_rsqf(var + 1e-5f);
        LD(f32x2, L_RST + row * 8) = (f32x2){rs, -mu * rs};
    }
    qkv_issue(4, 0);
    unpack(0, 5);
    qkv_issue(5, 24 * K);
    unpack(5, 10);
    u32x4 ma, mc;
    mod_ld(w, 8 + w, ma, mc);
    VMW(11); TIE1(pfd); BAR();
    STAMP(5);
    bf16x8 Wq[8][3];
    const unsigned trq = kh * 12288 + g * 3072 + q * 128 + (((2 * hd) ^ (fl << 1)) + (p >> 1)) * 16 + (p & 1) * 8;
    unsigned trqh = trq + 72 * K; PIN(trqh);
    auto qkv_read = [&](int r, unsigned base, unsigned slot) AI {
#pragma unroll
        for (int m = 0; m < 3; ++m) Wq[r][m] = TRF(base + slot + m * 1024, 512);
    };
    unsigned ra = L_RST + r8 * 8; PIN(ra);
    auto nrm = [&](int n) AI {
        const f32x2 st = LD(f32x2, ra + n * 64);
#pragma unroll
        for (int j = 0; j < 8; ++j) { t[n][j] = __builtin_fmaf(t[n][j], st.x, st.y); PIN(t[n][j]); }
    };
    qkv_read(0, trq, 48 * K); qkv_read(1, trqh, 0); qkv_read(2, trqh, 48 * K);
    nrm(0); nrm(1);
    BAR();
    STAMP(6);
    qkv_issue(6, 48 * K);
    nrm(2); nrm(3); nrm(4); nrm(5);
    qkv_issue(7, 72 * K);
    nrm(6); nrm(7); nrm(8); nrm(9);
    const unsigned tile0 = cg * 11 > 75 ? 75 : cg * 11;
    const unsigned hrot = (hd - rot) & 3;
    STAMP(7);
    auto body = [&](auto KHC) AI {
    constexpr bool A = decltype(KHC)::v == 0, B = !A;
    constexpr unsigned KH = decltype(KHC)::v;
    if (B) __builtin_amdgcn_s_setprio(1);
    bf16x8 Wo[4][4], W1[8][3], W2[6][4];
    unsigned v1o[3];
    const unsigned f1 = hd == 3 ? 8 : 3 * hd;
    auto v1o_mk = [&]() AI {
#pragma unroll
        for (int j = 0; j < 3; ++j) { const unsigned ch = (f1 + j) * 64 + lane, r = ch / 22; v1o[j] = r * 2752 + (ch - r * 22) * 16 - j * 1024; PIN(v1o[j]); }
    };
    auto w1_issue = [&](int h2, int r, unsigned slot) AI {
        dma3v(r1, LB + slot + f1 * 1024, v1o[0], v1o[1], v1o[2], (h2 * 8 + ((r + 2 * rot) & 7)) * (32 * 2752) + tile0 * 32);
    };
    auto w1_one = [&](int h2, int r, unsigned slot, auto jc) AI {
        CI(j, jc);
        dma1o<j * 1024>(r1, LB + slot + f1 * 1024, v1o[j], (h2 * 8 + ((r + 2 * rot) & 7)) * (32 * 2752) + tile0 * 32);
    };
    auto wo_one = [&](int r, auto jc) AI {
        CI(j, jc);
        dma1o<j * 1024>(ro, LB + r * 16384 + hd * 4096, (j & 1) ? vo1k1 : vo1k0, (hd * 4 + r) * 32768 + cg * 128 + j * 7168);
    };
    auto row_read = [&](bf16x8* W, unsigned base) AI {
        W[0] = TRF(base + tre, 512); W[1] = TRF(base + tro, 512); W[2] = TRF(base + (tre ^ 64), 512); W[3] = TRF(base + (tro ^ 64), 512);
    };
    unsigned tr1;
    auto w1_read = [&](int r, unsigned slot) AI {
        unsigned ba = tr1; if (slot >= 64 * K) { ba += 64 * K; PIN(ba); slot -= 64 * K; }
#pragma unroll
        for (int m = 0; m < 3; ++m) W1[r][m] = TRF(ba + slot + m * 32, 5632);
    };
    unsigned aw0 = L_H + ((2 * (KH * 4 + hrot) + (c8 >> 2)) * 64 + (c8 & 3) * 16 + (r8 ^ ((c8 & 3) | ((c8 >> 2) * 12)))) * 16, aw1 = aw0 ^ 128;
    unsigned ar0 = L_H + (KH * 512 + g * 16 + (tk ^ g)) * 16, ar1 = L_H + (KH * 512 + 64 + g * 16 + (tk ^ (g | 12))) * 16;
    unsigned xp = L_X + hd * 3072 + l16;
    PIN(aw0); PIN(aw1); PIN(ar0); PIN(ar1); PIN(xp);
    auto pref = [&](int n) AI {
        const unsigned j = b >> 3, hf_ = b & 1;
        if (n == 0) touch(pfd, r1, tk < 12 ? g * 2752 + tk * 128 : OOB, (j * 16 + hd * 4) * 2752 + hf_ * 1344);
        else touch(pfd, r2, (lane >> 2) * 1024 + p * 128, hd < 3 ? (j * 43 + hd * 16) * 1024 + hf_ * 512 : OOB);
    };
    VMW(6);
    STAMP(8);
    f32x4 A0, A1, S0, S1;
    {
        const unsigned r = mod_fin(w, 8 + w, ma, mc, 1.f);
        LD(f32x2, L_AS + w * 512 + (g & 1) * 256 + tk * 16 + (g >> 1) * 8) = (f32x2){lo16(r), hi16(r)};
        unsigned aa = L_AS + w * 512 + c8 * 32; PIN(aa);
        S0 = LD(f32x4, aa); S1 = LD(f32x4, aa + 16); A0 = LD(f32x4, aa + 256); A1 = LD(f32x4, aa + 272);
    }
    STAMP(9);
    u32x4 hw;
    auto norm1 = [&](auto ic) AI {
        constexpr int n = decltype(ic)::v >> 2, j = decltype(ic)::v & 3;
        const f32x4 Av = j < 2 ? A0 : A1, Sv = j < 2 ? S0 : S1;
        hw[j] = pk2(__builtin_fmaf(t[n][2 * j], Av[(2 * j) & 3], Sv[(2 * j) & 3]), __builtin_fmaf(t[n][2 * j + 1], Av[(2 * j + 1) & 3], Sv[(2 * j + 1) & 3]));
        IF (j == 3) LD(u32x4, ((n >> 1) & 1) * 16384 + ((n & 1) ? aw1 : aw0)) = hw;
    };
    sfor<0, 8>(norm1);
    BAR();
    STAMP(10);
    bf16x8 hf[8];
    auto h_rd = [&](int s, unsigned base) AI { hf[s] = LD(bf16x8, base + (s >> 1) * 2048 + ((s & 1) ? ar1 : ar0)); };

    f32x4 aq, ak, av, oq, ok, ov;
    u32x2 kc, vc, kp, vp;
    f32x4 pq_, pk_, pv_, mcur, mprev, sp, sc, sm, ao; u32x4 M, ones, P; unsigned q0, q1, mkp; float t0, t1, mx, inv;
    auto attn = [&](auto Tc, auto ic) AI {
        CI(T, Tc); CI(i, ic);
        const unsigned xb = xp + (T & 1) * 12288;
        constexpr float C = 0.25f * 1.44269504089f;
        if (T == 0) {
            if (i == 0) { pk_ = LD(f32x4, xb + 1024); pv_ = LD(f32x4, xb + 2048); }
            if (i == 5) kc = (u32x2){pk2(ok[0] + pk_[0], ok[1] + pk_[1]), pk2(ok[2] + pk_[2], ok[3] + pk_[3])};
            if (i == 7) vc = (u32x2){pk2(ov[0] + pv_[0], ov[1] + pv_[1]), pk2(ov[2] + pv_[2], ov[3] + pv_[3])};
            return;
        }
        if (i == 0) { pq_ = LD(f32x4, xb); mprev = LD(f32x4, mkp + 1024); pk_ = LD(f32x4, xb + 1024); mcur = LD(f32x4, mkp); pv_ = LD(f32x4, xb + 2048); kp = kc; vp = vc; }
        if (i == 4) {
            if (T == 1) { const float ni = rg == 0 ? -__builtin_inff() : 0.f; mprev += ni; }
            oq += pq_; q0 = pk2(oq[0], oq[1]); q1 = pk2(oq[2], oq[3]); sp = mfma16u(kp[0], kp[1], q0, q1, mprev);
        }
        if (i == 5) { ok += pk_; kc = (u32x2){pk2(ok[0], ok[1]), pk2(ok[2], ok[3])}; sc = mfma16u(kc[0], kc[1], q0, q1, mcur); M = LD(u32x4, mkp + 2048); ones = LD(u32x4, mkp + 3072); }
        if (i == 6) { ov += pv_; vc = (u32x2){pk2(ov[0], ov[1]), pk2(ov[2], ov[3])}; }
        if (i == 7) { t0 = fmx(fmx(fmx(sp[0], sp[1]), sc[0]), sc[1]); t1 = fmx(fmx(fmx(sp[2], sp[3]), sc[2]), sc[3]); }
        if (i == 8) mx = xmax16(fmx(t0, t1));
        if (i == 9) mx = xmax32(mx) * C;
        if (i == 10) { t0 = __builtin_amdgcn_exp2f(__builtin_fmaf(t0, C, -mx)); t1 = __builtin_amdgcn_exp2f(__builtin_fmaf(t1, C, -mx)); }
        if (i == 11) { q0 = pk2(t0, t0); q1 = pk2(t1, t1); P = (u32x4){q0 & M[0], q1 & M[1], q0 & M[2], q1 & M[3]}; }
        if (i == 12) { sm = mfma32u(ones, P, (f32x4){0, 0, 0, 0}); ao = mfma32u((u32x4){vp[0], vp[1], vc[0], vc[1]}, P, (f32x4){0, 0, 0, 0}); }
        if (i == 14) inv = __builtin_amdgcn_rcpf(sm[0]);
        if (i == 15) { ao *= inv; q0 = pk2(ao[0], ao[1]); q1 = pk2(ao[2], ao[3]); }
        if (i == 16) {
            auto ra_ = __builtin_amdgcn_permlane16_swap(q0, q0, false, false);
            auto rb_ = __builtin_amdgcn_permlane16_swap(q1, q1, false, false);
            st16s(rw, vos, WS_O + ((rg * 4 + T - 1) * 16 + cg * 2) * 1024 + hd * 512, (u32x4){ra_[0], rb_[0], ra_[1], rb_[1]});
        }
    };
    const u32x4 ff = {~0u, ~0u, ~0u, ~0u};
    auto clr = [&](int n) AI {
        const unsigned vw = n < 6 ? hd : hd + 4; const int i = n < 6 ? n : n - 6;
        const unsigned ub = WS_U + (rg * 4 + (vw >> 1)) * 44032 + tile0 * 512 + (vw & 1) * 3072;
        const unsigned lo32 = lane < 32 ? 0u : OOB;
        if (i == 0) st16s(rw, l16, WS_O + ((rg * 4 + (vw >> 1)) * 16 + cg * 2 + (vw & 1)) * 1024, ff);
        if (i == 1) st16s(rw, vy, WS_Y + (rg * 64 + vw * 8) * 1024 + cg * 128, ff);
        if (i == 2) st16s(rw, l16, (cg < 7 || (vw & 1)) ? ub : OOB, ff);
        if (i == 3) st16s(rw, l16, ub + 1024, ff);
        if (i == 4) st16s(rw, l16 | ((vw & 1) ? lo32 : 0u), ub + 2048, ff);
        if (i == 5) st16s(rw, vy | lo32, vw == 0 ? WS_ST + (rg * 32 + cg) * 128 : OOB, ff);
    };
    u32x4 m2a, m2c, m3a, m3c, m4a, m4c; unsigned flg;
    u32x4 ofb[2][4], fa;
    const unsigned tg0 = b == 0 ? tag : 0u;
    auto fl_ld = [&]() AI { flg = ld4c(rw, (lane & 31) * 4, WS_FLAG + rg * 128); fa = ld16c<0>(rw, l16, b == 0 ? WS_FLAG + hd * 1024 : OOB); };
    auto o_ld = [&](int tt) AI {
        const unsigned so = WS_O + ((rg * 4 + tt) * 16 + 4 * hd) * 1024;
        u32x4* of = ofb[tt & 1];
        of[0] = ld16c<0>(rw, l16, so); of[1] = ld16c<1024>(rw, l16, so); of[2] = ld16c<2048>(rw, l16, so); of[3] = ld16c<3072>(rw, l16, so);
    };
    auto cbw = [&]() AI { return (g & 1) * 1024 + (tk >> 3) * 64 + (tk & 3) * 16 + ((tk >> 2) & 1) * 8 + (g >> 1) * 4; };
    u32x2 xres[4]; unsigned fvo;
    auto qside = [&](auto Tc, auto ic) AI {
        CI(T, Tc); CI(i, ic);
        constexpr unsigned HN = ((T + 1) & 1) * 16384;
        constexpr int BI = T == 0 ? 7 : 4;
        IF (T == 0 && i < 2) { norm1(IC<8 + 2 * i>{}); norm1(IC<9 + 2 * i>{}); }
        IF (T == 0 && i >= 2 && i < 14) norm1(IC<10 + i>{});
        IF (T == 1 && i >= 6 && i < 22 && !(i & 1)) norm1(IC<24 + (i - 6) / 2>{});
        IF (T == 2 && i < 3) norm1(IC<32 + i>{});
        IF (T == 2 && i >= 5 && i < 8) norm1(IC<30 + i>{});
        IF (T == 2 && A && (i == 8 || i == 9)) norm1(IC<30 + i>{});
        IF (T == 2 && B && (i == 21 || i == 22)) norm1(IC<17 + i>{});
        if (A) {
            const unsigned xb = xp + ((T - 1) & 1) * 12288;
            if (T > 1 && i == 0) LD(f32x4, xb) = oq;
            if (T > 0 && i == 1) LD(f32x4, xb + 1024) = ok;
            if (T > 0 && i == 2) LD(f32x4, xb + 2048) = ov;
            IF (T == 1 && i % 3 == 1) wo_one(i / 12, IC<(i / 3) & 3>{});
            IF (T == 2 && i < 16 && !(i & 1)) wo_one(2 + i / 8, IC<(i / 2) & 3>{});
            IF (T == 2 && i >= 16 && i < 22 && !(i & 1)) w1_one(1, 0, Z5, IC<(i - 16) / 2>{});
            if (T == 2 && i == 3) { VMW(6); row_read(Wo[0], 0); }
            if (T == 2 && i == 11) { v1o_mk(); }
            if (T == 2 && i == 13) { VMW(7); row_read(Wo[1], 16384); }
            IF (T == 3 && i < 18 && !(i & 1)) w1_one(i < 12 ? 1 : 0, i < 6 ? 1 : i < 12 ? 2 : 0, i < 6 ? Z6 : i < 12 ? Z7 : Z0, IC<(i / 2) % 3>{});
            if (T == 3 && i == 1) { VMW(10); row_read(Wo[2], 32768); }
            if (T == 3 && i == 9) { VMW(10); row_read(Wo[3], 49152); }
            IF (T == 4 && i < 6 && !(i & 1)) w1_one(0, 1, Z1, IC<i / 2>{});
            if (T == 4 && i == 4) VMW(3);
            if (T == 4 && i == 5) fl_ld();
            IF (T == 4 && i >= 7 && i < 16) w1_one(0, 2 + (i - 7) / 3, i < 10 ? Z2 : i < 13 ? Z3 : Z4, IC<(i - 7) % 3>{});
            if (T == 4 && i == 6) w1_read(0, Z0);
            if (T == 4 && i == 21) o_ld(1);
            if (T == 4 && i == 18) {
                VMW(9); TIE2(flg, fa);
#pragma nounroll
                for (int it = 0; it < SPIN && anyl(((flg ^ tag) | (fa[0] ^ tg0) | (fa[1] ^ tg0) | (fa[2] ^ tg0) | (fa[3] ^ tg0)) != 0); ++it) { fl_ld(); VMW(0); TIE2(flg, fa); }
                o_ld(0);
            }
            if (T == 0 && i == 7) VMW(0);
            if (T == 2 && i == 22) mod_ld(16 + cg, 40 + cg, m3a, m3c);
            if (T == 3 && i == 13) { VMW(7); const unsigned r = mod_fin(16 + cg, 40 + cg, m3a, m3c, 0.f); LD(unsigned, L_GT + cbw() - (g & 1) * 896) = r; }
            if (T == 3 && i == 15) mod_ld(28 + hd, 36 + hd, m4a, m4c);
            if (T == 4 && i == 16) LD(unsigned, L_CB + 512 + hrot * 128 + cbw()) = mod_fin(28 + hd, 36 + hd, m4a, m4c, 1.f);
        } else {
            if (T == 0 && !(i & 1)) clr(i / 2);
            if (T == 1 && i < 6 && !(i & 1)) clr(8 + i / 2);
            if (T == 0 && i == 7) VMW(4);
            if (T == 1 && i == 7) LD(unsigned, L_CB + hrot * 128 + cbw()) = mod_fin(24 + hd, 32 + hd, m2a, m2c, 1.f);
            if ((PF & 2) && T == PT1 && i == 9) pref(0);
            if ((PF & 8) && T == PT2 && i == 11) pref(1);
            if (T == 2 && i == 3) { VMW(0); if (PT2 < 2) TIE1(pfd); st4s(rw, lane == 0 ? 0u : OOB, WS_FLAG + rg * 128 + cg * 16 + hd * 4, tag); }
            IF (T > 0 && i > 4) attn(IC<T - 1>{}, IC<i - 4>{});
            if (T == 4 && i == 6) {
                fvo = (hd * 4 + g) * 1024 + ((lane >> 3) & 1) * 64 + (lane & 1) * 32 + ((lane >> 1) & 3) * 8; PIN(fvo);
#pragma unroll
                for (int tt = 0; tt < 4; ++tt) xres[tt] = ld8(rx, fvo, (rg * 64 + tt * 16) * 1024 + cg * 128);
            }
            if (T == 4 && i == 13) w1_read(0, Z5);
            if (T == 4 && i == 17) w1_read(1, Z6);
            if (T == 4 && i == 21) w1_read(2, Z7);
        }
        if (i == BI) {
            STAMP(40 + 2 * T);
            BAR();
            STAMP(41 + 2 * T);
            IF (B && T > 0) attn(IC<T - 1>{}, IC<0>{});
        }
        IF (T == 0) {
            if (i == 0) { qkv_read(4, trq, 0); }
            if (i == 2) { qkv_read(5, trq, 24 * K); }
            if (i == 7) { qkv_read(6, trq, 48 * K); }
            if (i == 8) { qkv_read(7, trqh, 0); h_rd(0, HN); h_rd(1, HN); }
            if (i == 9) { h_rd(2, HN); h_rd(3, HN); h_rd(4, HN); }
            if (i == 11) h_rd(5, HN);
            if (i == 13) h_rd(6, HN);
            if (i == 15) h_rd(7, HN);
        }
        IF (T > 0 && T < 4) {
            if (i == 5) h_rd(0, HN);
            if (i >= 5 && i % 3 == 2) h_rd(i / 3, HN);
        }
        SB();
    };
    if (B) { mkp = L_MK + l16; PIN(mkp); PIN(vos); mod_ld(24 + hd, 32 + hd, m2a, m2c); }
    tr1 = (4 * g + q) * 352 + hd * 96 + p * 8; PIN(tr1);
    h_rd(0, 0); h_rd(1, 0); h_rd(2, 0); h_rd(3, 0); qkv_read(3, trqh, 24 * K); h_rd(4, 0); h_rd(5, 0); h_rd(6, 0); h_rd(7, 0);
    sfor<0, 5>([&](auto Tc) AI {
        CI(T, Tc);
        oq = aq; ok = ak; ov = av;
        aq = (f32x4){0, 0, 0, 0}; ak = aq; av = aq;
        SB();
        sfor<0, 8>([&](auto sc_) AI {
            CI(s, sc_);
            IF (T > 0) { aq = mfma32(Wq[s][0], hf[s], aq); qside(Tc, IC<3 * s>{}); }
            ak = mfma32(Wq[s][1], hf[s], ak); qside(Tc, IC<(T > 0 ? 3 * s + 1 : 2 * s)>{});
            av = mfma32(hf[s], Wq[s][2], av); qside(Tc, IC<(T > 0 ? 3 * s + 2 : 2 * s + 1)>{});
        });
        STAMP(11 + T);
    });
    if (A) { LD(f32x4, xp) = aq; LD(f32x4, xp + 1024) = ak; LD(f32x4, xp + 2048) = av; }
    oq = aq; ok = ak; ov = av;
    BAR();

    STAMP(56);
    if (B) {
        sfor<0, 17>([&](auto ic) AI { attn(IC<4>{}, ic); });
        if (b == 0 && w == 4) st4s(rw, lane == 0 ? 0u : OOB, WS_EPOCH, tag);
        SB();
    }
    STAMP(50);
    RELANE();
    const unsigned fb = (lane >> 3) & 1, fg = (lane >> 1) & 3, fe = lane & 1, ftok = hd * 4 + g;
    unsigned pw0 = w * 4096 + tk * 256 + (g ^ (tk & 7)) * 16;
    unsigned pr = ftok * 256 + ((fb * 8 + fe * 4 + fg) ^ (ftok & 7)) * 16;
    auto w2_issue = [&](unsigned vw, int s, unsigned slot) AI {
        const unsigned ks = vw * 5 + (vw < 3 ? vw : 3) + s;
        dma4(r2, LB + slot + vw * 4096, vo1k0, vo1k1, ks * 32768 + cg * 128);
    };
    auto w2_one = [&](unsigned vw, int s, unsigned slot, auto jc) AI {
        CI(j, jc);
        const unsigned ks = vw * 5 + (vw < 3 ? vw : 3) + s;
        dma1o<j * 1024>(r2, LB + slot + vw * 4096, (j & 1) ? vo1k1 : vo1k0, ks * 32768 + cg * 128 + j * 7168);
    };
    u32x2 x1p[4];
    f32x4 sacc[3] = {{0, 0, 0, 0}, {0, 0, 0, 0}, {0, 0, 0, 0}};
    auto nof = [&](auto) AI {};
    auto s1_part = [&](auto s0c, auto s1c, auto side) AI {
        CI(s0, s0c); CI(s1, s1c);
        unsigned ca = L_CB + (tk & 1) * 1024 + KH * 512 + g * 16; PIN(ca);
        bf16x8 yf[8];
#pragma unroll
        for (int s = s0; s < s1; ++s) yf[s] = LD(bf16x8, ca + s * 64);
        sfor<s0, s1>([&](auto sc_) AI {
            CI(s, sc_);
            side(IC<s - s0>{});
#pragma unroll
            for (int m = 0; m < 3; ++m) sacc[m] = mfma32(W1[s][m], yf[s], sacc[m]);
            SB();
        });
    };
    u32x4 yv[4], sv;
    auto y_ld = [&](int tt) AI {
        const unsigned so = WS_Y + (rg * 4 + tt) * 16384 + hd * 128;
        yv[0] = ld16c<0>(rw, vy, so); yv[1] = ld16c<0>(rw, vy, so + 8192); yv[2] = ld16c<512>(rw, vy, so); yv[3] = ld16c<512>(rw, vy, so + 8192);
        sv = ld16c<0>(rw, l16, hd == (unsigned)tt ? WS_ST + (rg * 4 + tt) * 1024 : OOB);
    };
    aw0 += L_ACT - L_H; aw1 += L_ACT - L_H; ar0 += L_ACT - L_H; ar1 += L_ACT - L_H; PIN(ar0); PIN(ar1);
    if (A) { PIN(aw0); PIN(aw1); }
    auto y_put = [&](int tt) AI {
        TIE2(yv[0], yv[1]); TIE2(yv[2], yv[3]); TIE1(sv);
#pragma nounroll
        for (int it = 0; it < SPIN && anyl(umax4(sv, umax4(yv[3], umax4(yv[2], umax4(yv[1], umax4(yv[0], 0))))) == ~0u); ++it) {
            y_ld(tt); VMW(0); TIE2(yv[0], yv[1]); TIE2(yv[2], yv[3]); TIE1(sv);
        }
        LD(u32x4, (tt & 1) * 16384 + aw0) = yv[0]; LD(u32x4, (tt & 1) * 16384 + aw1) = yv[1];
        LD(u32x4, (tt & 1) * 16384 + 8192 + aw0) = yv[2]; LD(u32x4, (tt & 1) * 16384 + 8192 + aw1) = yv[3];
    };
    auto y_stat = [&](int tt) AI {
        if (hd == (unsigned)tt) {
            float s = sw16add(U2F(sv[0]), U2F(sv[2])), qq = sw16add(U2F(sv[1]), U2F(sv[3]));
            s += dpp<0x128>(s); qq += dpp<0x128>(qq);
            s = sw32add(s, s); qq = sw32add(qq, qq);
            const float mu = s * (1.f / 512.f);
            const float var = __builtin_fmaxf(qq * (1.f / 512.f) - mu * mu, 0.f);
            LD(f32x2, L_SST + tt * 128 + (c8 * 2 + (g & 1)) * 8) = (f32x2){-mu, -1.44269504089f * __builtin_amdgcn_rsqf(var + 1e-5f)};
        }
    };
    if (A) {
        unsigned pa0 = L_PW + pw0, pa1 = pa0 ^ 64; PIN(pa0); PIN(pa1);
        auto wo_step = [&](int tt, auto side) AI {
            u32x4* of = ofb[tt & 1];
            TIE2(of[0], of[1]); TIE2(of[2], of[3]);
#pragma nounroll
            for (int it = 0; it < SPIN && anyl(umax4(of[3], umax4(of[2], umax4(of[1], umax4(of[0], 0)))) == ~0u); ++it) { o_ld(tt); VMW(0); TIE2(of[0], of[1]); TIE2(of[2], of[3]); }
            STAMP(16 + tt * 2);
            f32x4 acc[4] = {{0, 0, 0, 0}, {0, 0, 0, 0}, {0, 0, 0, 0}, {0, 0, 0, 0}};
            sfor<0, 16>([&](auto ic) AI {
                CI(i, ic); constexpr int c = i >> 2, j = i & 3;
                acc[c] = mfma32(Wo[j][c], __builtin_bit_cast(bf16x8, of[j]), acc[c]);
                IF (c > 0 && j == 2) LD(f32x4, (tt & 1) * PWD + ((c - 1) >> 1) * 128 + (((c - 1) & 1) ? pa1 : pa0)) = acc[c - 1];
                IF (i & 1) side(IC<i / 2>{});
                SB();
            });
            LD(f32x4, (tt & 1) * PWD + 128 + pa1) = acc[3];
            SB();
        };
        auto o_ld1 = [&](int tt, auto jc) AI { CI(j, jc); ofb[tt & 1][j] = ld16c<j * 1024>(rw, l16, WS_O + ((rg * 4 + tt) * 16 + 4 * hd) * 1024); };
        w1_issue(1, 3, Z5);
        w1_read(1, Z1);
        VMW(7); wo_step(0, [&](auto ic) AI { CI(i, ic); IF (i < 6) w1_one(1, 4 + i / 3, i < 3 ? Z6 : Z7, IC<i % 3>{}); });
        XS(2);
        BAR();
        STAMP(17);
        w1_read(2, Z2); w1_read(3, Z3); w1_read(4, Z4);
        VMW(9); wo_step(1, [&](auto ic) AI { CI(i, ic); IF (i < 4) w2_one(hd, 0, 0, ic); else o_ld1(2, IC<i - 4>{}); });
        XS(3);
        s1_part(IC<0>{}, IC<2>{}, nof);
        VMW(8);
        XS(4);
        BAR();
        STAMP(19);
        s1_part(IC<2>{}, IC<5>{}, [&](auto ic) AI { w2_one(hd + 4, 0, 0, ic); });
        VMW(3); wo_step(2, [&](auto ic) AI { CI(i, ic); IF (i == 0) w2_one(hd + 4, 0, 0, IC<3>{}); IF (i >= 4) o_ld1(3, IC<i - 4>{}); });
        XS(6);
        BAR();
        STAMP(21);
        w1_read(5, Z8); w1_read(6, Z9);
        y_ld(0);
        s1_part(IC<5>{}, IC<7>{}, nof);
        XS(7);
        VMW(5); wo_step(3, [&](auto ic) AI { CI(i, ic); IF (i < 4) w2_one(hd + 4, 4, 64 * K, ic); });
        XS(8);
        BAR();
        STAMP(23);
        w1_read(7, Z3);
        VMW(4); y_put(0);
        STAMP(51);
        y_stat(0);
        y_ld(1);
        s1_part(IC<7>{}, IC<8>{}, nof);
#pragma unroll
        for (int m = 0; m < 3; ++m) LD(f32x4, L_S1 + (tk & 1) * 768 + (hd * 48 + m * 16 + g * 4) * 4) = sacc[m];
        XS(57);
    } else {
        unsigned prw = L_PW + pr; PIN(prw);
        f32x4 ga, A2;
        auto fin_wo = [&](int tt, auto side) AI {
            f32x4 pp[4];
#pragma unroll
            for (int ww = 0; ww < 4; ++ww) pp[ww] = LD(f32x4, prw + (tt & 1) * PWD + ww * 4096);
            if (tt == 0) {
                const u32x2 gv = LD(u32x2, L_GT + fb * 64 + fg * 16 + fe * 8);
                const u32x2 av2 = LD(u32x2, L_CB + 1024 + ((cg & 4) + ((cg - rot) & 3)) * 128 + fb * 64 + fg * 16 + fe * 8);
                ga = (f32x4){lo16(gv[0]), hi16(gv[0]), lo16(gv[1]), hi16(gv[1])};
                A2 = (f32x4){lo16(av2[0]), hi16(av2[0]), lo16(av2[1]), hi16(av2[1])};
            }
            side(IC<0>{});
            const u32x2 xr2 = xres[tt];
            const f32x4 xv = {lo16(xr2[0]), hi16(xr2[0]), lo16(xr2[1]), hi16(xr2[1])};
            const f32x4 sum = (pp[0] + pp[1]) + (pp[2] + pp[3]);
            const f32x4 v = ga * sum + xv;
            const f32x4 xx = v * A2;
            const unsigned y0 = pk2(xx[0], xx[1]), y1 = pk2(xx[2], xx[3]);
            const u32x4 yy = {y0, y1, dppu<0xF5>(y0), dppu<0xF5>(y1)};
            st16s(rw, fe ? OOB : ftok * 1024 + tk * 8, WS_Y + (rg * 4 + tt) * 16384 + cg * 128, yy);
            side(IC<1>{});
            const u32x2 x1 = {pk2(v[0], v[1]), pk2(v[2], v[3])};
            x1p[tt] = x1;
            float s = dot2(x1[1], 0x3f803f80u, dot2(x1[0], 0x3f803f80u, 0.f));
            const float qq = dot2(x1[1], x1[1], dot2(x1[0], x1[0], 0.f));
            asm volatile("s_nop 1\n\tv_add_f32_dpp %0, %0, %0 row_ror:8 row_mask:0xf bank_mask:0x3\n\tv_add_f32_dpp %0, %1, %1 row_ror:8 row_mask:0xf bank_mask:0xc\n\ts_nop 1" : "+v"(s) : "v"(qq));
            s += dpp<0x141>(s); s += dpp<0xB1>(s); s += dpp<0x4E>(s);
            st4s(rw, c8 ? OOB : ftok * 8 + fb * 4, WS_ST + ((rg * 4 + tt) * 8 + cg) * 128, F2U(s));
            side(IC<2>{});
            SB();
        };
        v1o_mk();
        VMW(2); if (PT2 >= 2) TIE1(pfd); TIE2(xres[0], xres[1]); TIE2(xres[2], xres[3]);
        XS(2);
        BAR();
        STAMP(17);
        fin_wo(0, [&](auto ic) AI { w1_one(0, 5, Z8, ic); });
        STAMP(18);
        s1_part(IC<0>{}, IC<3>{}, [&](auto ic) AI { w1_one(0, 6, Z9, ic); });
        XS(4);
        BAR();
        STAMP(19);
        fin_wo(1, [&](auto ic) AI { w1_one(0, 7, Z3, ic); });
        STAMP(20);
        w1_read(3, Z5); w1_one(1, 6, Z4, IC<0>{}); w1_read(4, Z6); w1_one(1, 6, Z4, IC<1>{}); w1_read(5, Z7); w1_one(1, 6, Z4, IC<2>{});
        XS(5);
        VMW(8);
        XS(6);
        BAR();
        STAMP(21);
        w1_issue(1, 7, Z5);
        fin_wo(2, nof);
        STAMP(22);
        s1_part(IC<3>{}, IC<6>{}, nof);
        XS(7);
        VMW(2);
        XS(8);
        BAR();
        STAMP(23);
        w1_read(6, Z4); w1_read(7, Z5);
        fin_wo(3, nof);
        STAMP(51);
        s1_part(IC<6>{}, IC<8>{}, nof);
        XS(57);
    }
    BAR();
    STAMP(24);

    h_rd(0, 0); h_rd(1, 0); h_rd(2, 0); h_rd(3, 0); h_rd(4, 0); h_rd(5, 0); h_rd(6, 0); h_rd(7, 0);
    unsigned xp1 = L_XC1 + hd * 3072 + l16; PIN(xp1);
    unsigned s1a = L_S1 + hd * 192 + g * 16;
    unsigned vus0 = (g >> 1) * 256 + tk * 16 + (g & 1) * 512;
    unsigned ssa = L_SST + tk * 8;
    if (B) { PIN(s1a); PIN(vus0); PIN(ssa); PIN(vos); }
    const unsigned ks0 = w * 5 + (w < 3 ? w : 3);
    u32x4 uf[3][6];
    auto u_ld1 = [&](int tt, auto jc) AI {
        CI(j, jc);
        const unsigned so = WS_U + ((rg * 4 + tt) * 43 + ks0) * 1024;
        IF (j < 4) uf[tt % 3][j] = ld16c<j * 1024>(rw, l16, so);
        IF (j == 4) uf[tt % 3][4] = ld16c<0>(rw, l16, so + 4096);
        IF (j == 5 && A) uf[tt % 3][5] = ld16c<0>(rw, l16, w < 3 ? so + 5120 : OOB);
    };
    auto u_ld = [&](int tt) AI {
        const unsigned so = WS_U + ((rg * 4 + tt) * 43 + ks0) * 1024;
        u32x4* u = uf[tt % 3];
        u[0] = ld16c<0>(rw, l16, so); u[1] = ld16c<1024>(rw, l16, so); u[2] = ld16c<2048>(rw, l16, so); u[3] = ld16c<3072>(rw, l16, so);
        u[4] = ld16c<0>(rw, l16, so + 4096);
        if (A) u[5] = ld16c<0>(rw, l16, w < 3 ? so + 5120 : OOB);
    };
    f32x4 acc[3], oa[3], pa[3], t1v[3], s1v[3], ev[3]; f32x2 st; unsigned ua[3], ub[3];
    auto silu = [&](auto tc, auto ic) AI {
        CI(tt, tc); CI(i, ic);
        const unsigned xb = xp1 + (tt & 1) * 12288;
        auto Lm = [&](int m) AI { s1v[m] = LD(f32x4, s1a + 768 + m * 64); t1v[m] = LD(f32x4, s1a + m * 64); };
        auto Pm = [&](int m) AI {
#pragma unroll
            for (int r = 0; r < 4; ++r) oa[m][r] = __builtin_fmaf(st.y, __builtin_fmaf(st.x, s1v[m][r], oa[m][r]), t1v[m][r]);
        };
        auto Zm = [&](int m) AI {
#pragma unroll
            for (int r = 0; r < 4; ++r) oa[m][r] = __builtin_fmaf(st.y, pa[m][r], oa[m][r]);
        };
        auto Em = [&](int m) AI {
#pragma unroll
            for (int r = 0; r < 4; ++r) ev[m][r] = __builtin_amdgcn_exp2f(oa[m][r]);
        };
        auto Cm = [&](int m) AI {
#pragma unroll
            for (int r = 0; r < 4; ++r) ev[m][r] = __builtin_amdgcn_rcpf(ev[m][r]);
        };
        auto Fm = [&](int m) AI { oa[m] *= ev[m]; ua[m] = pk2(oa[m][0], oa[m][1]); ub[m] = pk2(oa[m][2], oa[m][3]); };
        if (i == 0) { st = LD(f32x2, ssa + tt * 128); Lm(0); Lm(1); }
        if (i == 3) { Pm(0); }
        if (i == 4) { pa[0] = LD(f32x4, xb); pa[1] = LD(f32x4, xb + 1024); Lm(2); }
        if (i == 5) Pm(1);
        if (i == 6) pa[2] = LD(f32x4, xb + 2048);
        if (i == 7) Zm(0);
        if (i == 8) Em(0);
        if (i == 9) { Zm(1); ev[0] += 1.f; }
        if (i == 10) Cm(0);
        if (i == 11) Em(1);
        if (i == 12) { Fm(0); ev[1] += 1.f; }
        if (i == 13) Cm(1);
        if (i == 14) Pm(2);
        if (i == 15) Fm(1);
        const unsigned so = WS_U + (rg * 4 + tt) * 44032 + (tile0 + hd * 3) * 512;
        if (i == 16) {
            auto ra_ = __builtin_amdgcn_permlane16_swap(ua[0], ua[1], false, false);
            auto rb_ = __builtin_amdgcn_permlane16_swap(ub[0], ub[1], false, false);
            st16s(rw, vus0, (cg < 7 || hd > 0) ? so : OOB, (u32x4){ra_[0], rb_[0], ra_[1], rb_[1]});
            Zm(2);
        }
        if (i == 17) Em(2);
        if (i == 18) ev[2] += 1.f;
        if (i == 19) Cm(2);
        if (i == 20) Fm(2);
        if (i == 21) {
            auto ra_ = __builtin_amdgcn_permlane16_swap(ua[2], ua[2], false, false);
            auto rb_ = __builtin_amdgcn_permlane16_swap(ub[2], ub[2], false, false);
            st16s(rw, vos, hd < 3 ? so + 1024 : OOB, (u32x4){ra_[0], rb_[0], ra_[1], rb_[1]});
        }
    };
    auto fside = [&](auto tc, auto ic) AI {
        CI(tt, tc); CI(i, ic);
        constexpr unsigned AN = ((tt + 1) & 1) * 16384;
        if (tt > 0 && i == 4) { STAMP(57 + tt); BAR(); }
        if (tt == 0 && i == 2) row_read(W2[0], 0);
        if (tt == 1 && i == 22) row_read(W2[1], 32 * K);
        if (B) {
            IF (tt > 0) silu(IC<tt - 1>{}, ic);
            if (tt == 0 && i == 6) {
                const float sc1 = (tk & 1) ? 1.f : -1.44269504089f;
#pragma unroll
                for (int m = 0; m < 3; ++m) {
                    const unsigned o = L_S1 + (tk & 1) * 768 + (hd * 48 + m * 16 + g * 4) * 4;
                    LD(f32x4, o) = (LD(f32x4, o) + sacc[m]) * sc1;
                }
            }
        } else {
            IF (tt > 0 && i < 3) LD(f32x4, xp1 + ((tt - 1) & 1) * 12288 + i * 1024) = oa[i];
            IF (tt == 0 && i < 16 && !(i & 1)) w2_one(i < 8 ? hd : hd + 4, 1, 32 * K, IC<(i / 2) & 3>{});
            IF (tt == 1 && i < 16 && !(i & 1)) w2_one(i < 8 ? hd : hd + 4, 2, 0, IC<(i / 2) & 3>{});
            IF (tt == 2 && i < 8 && !(i & 1)) w2_one(hd, 5, 64 * K, IC<(i / 2) & 3>{});
            IF (tt == 2 && i >= 8 && i < 16 && !(i & 1)) w2_one(hd + 4, 3, 32 * K, IC<(i / 2) & 3>{});
            IF (tt == 3 && i < 16 && !(i & 1)) w2_one(hd, i < 8 ? 4 : 3, i < 8 ? 32 * K : 0, IC<(i / 2) & 3>{});
            if (tt == 0 && i == 5) { VMW(3); y_put(1); }
            if (tt == 0 && i == 7) { y_stat(1); y_ld(2); }
            if (tt == 0 && i == 21) { VMW(4); y_put(2); }
            if (tt == 0 && i == 23) { y_stat(2); y_ld(3); }
            if (tt == 1 && i == 3) VMW(7);
            if (tt == 1 && i == 19) { VMW(8); y_put(3); }
            if (tt == 1 && i == 21) y_stat(3);
            if (tt == 2 && i == 21) { VMW(8); row_read(W2[2], 0); }
        }
        if (tt == 0 && i == 8) { STAMP(52); BAR(); h_rd(0, AN); h_rd(1, AN); }
        if (tt > 0 && tt < 3 && i == 5) h_rd(0, AN);
        if (tt < 3 && i >= (tt ? 5 : 8) && i % 3 == 2) h_rd(i / 3, AN);
        if (tt == 3 && i == (A ? 20 : 22)) u_ld(0);
        SB();
    };
    sfor<0, 4>([&](auto tc) AI {
        CI(tt, tc);
#pragma unroll
        for (int m = 0; m < 3; ++m) { oa[m] = acc[m]; acc[m] = (f32x4){0, 0, 0, 0}; }
        SB();
        sfor<0, 24>([&](auto ic) AI { CI(i, ic); acc[i % 3] = mfma32(W1[i / 3][i % 3], hf[i / 3], acc[i % 3]); fside(tc, ic); });
        STAMP(25 + tt);
    });

    if (A) {
#pragma unroll
        for (int m = 0; m < 3; ++m) LD(f32x4, xp1 + 12288 + m * 1024) = acc[m];
        VMW(14);
        BAR();
    } else {
#pragma unroll
        for (int m = 0; m < 3; ++m) oa[m] = acc[m];
        sfor<0, 4>([&](auto ic) AI { silu(IC<3>{}, ic); });
        BAR();
        silu(IC<3>{}, IC<4>{});
    }
    STAMP(29);
    RELANE();
    u_ld(1);
    if (A) { VMW(12); row_read(W2[3], 0); row_read(W2[4], 32 * K); unsigned tb = 64 * K; row_read(W2[5], tb); }
    else { row_read(W2[2], 0); silu(IC<3>{}, IC<5>{}); silu(IC<3>{}, IC<6>{}); row_read(W2[3], 32 * K); silu(IC<3>{}, IC<7>{}); silu(IC<3>{}, IC<8>{}); row_read(W2[4], 64 * K); }
    f32x4 gb;
    if (B) {
        const u32x2 gv = LD(u32x2, L_GT + 128 + fb * 64 + fg * 16 + fe * 8);
        gb = (f32x4){lo16(gv[0]), hi16(gv[0]), lo16(gv[1]), hi16(gv[1])} * (-1.f / 1.44269504089f);
    }
    unsigned pw1 = pw0 ^ 64; PIN(pw0); PIN(pw1); if (B) PIN(pr);
    f32x4 pp[8];
    auto fin_out = [&](auto tc, auto ic) AI {
        CI(tt, tc); CI(i, ic);
        IF (i < 2) {
#pragma unroll
            for (int ww = 4 * i; ww < 4 * i + 4; ++ww) pp[ww] = LD(f32x4, pr + (tt & 1) * 32768 + ww * 4096);
        }
        if (i == 5) pp[0] += pp[1];
        if (i == 6) pp[2] += pp[3];
        if (i == 7) pp[4] += pp[5];
        if (i == 8) pp[6] += pp[7];
        if (i == 9) pp[0] += pp[2];
        if (i == 10) pp[4] += pp[6];
        const u32x2 xp2 = x1p[tt];
        if (i == 11) { pp[0] += pp[4]; }
        if (i == 12) pp[1] = (f32x4){lo16(xp2[0]), hi16(xp2[0]), lo16(xp2[1]), hi16(xp2[1])};
        if (i == 13) pp[0] = gb * pp[0] + pp[1];
        if (i == 14) __builtin_amdgcn_raw_buffer_store_b64((u32x2){pk2(pp[0][0], pp[0][1]), pk2(pp[0][2], pp[0][3])}, rout, (int)fvo, (int)((rg * 4 + tt) * 16384 + cg * 128), 0);
    };
    constexpr int NU = A ? 6 : 5;
    unsigned um;
    auto u_chk = [&](int tt, auto jc) AI {
        CI(j, jc);
        IF (j < NU) { TIE1(uf[tt % 3][j]); um = umax4(uf[tt % 3][j], j ? um : 0u); }
    };
    sfor<0, 4>([&](auto tc) AI {
        CI(tt, tc);
        u32x4* u = uf[tt % 3];
        IF (B && tt > 0) { fin_out(IC<tt - 1>{}, IC<0>{}); fin_out(IC<tt - 1>{}, IC<1>{}); }
        IF (tt == 0 || tt == 3) {
            VMW(tt == 0 ? NU : 0);
            sfor<0, 6>([&](auto jc) AI { u_chk(tt, jc); });
        }
#pragma nounroll
        for (int it = 0; it < SPIN && anyl(um == ~0u); ++it) {
            u_ld(tt); VMW(0);
            sfor<0, 6>([&](auto jc) AI { u_chk(tt, jc); });
        }
        STAMP(30 + tt * 2);
        f32x4 a2[4] = {{0, 0, 0, 0}, {0, 0, 0, 0}, {0, 0, 0, 0}, {0, 0, 0, 0}};
        auto pwr = [&](int c) AI { LD(f32x4, (tt & 1) * 32768 + (c >> 1) * 128 + ((c & 1) ? pw1 : pw0)) = a2[c]; };
        if (A) {
            if (w < 3) {
#pragma unroll
                for (int c = 0; c < 4; ++c) a2[c] = mfma32(W2[5][c], __builtin_bit_cast(bf16x8, u[5]), a2[c]);
            }
            SB();
            sfor<0, 20>([&](auto ic) AI {
                CI(i, ic); constexpr int c = i & 3, s = i >> 2;
                a2[c] = mfma32(W2[s][c], __builtin_bit_cast(bf16x8, u[s]), a2[c]);
                IF ((tt == 0 || tt == 2) && i % 3 == 0 && i < 18) u_ld1(tt ? 3 : 2, IC<i / 3>{});
                SB();
            });
            pwr(0); pwr(1); pwr(2); pwr(3);
            IF (tt < 2) { VMW(tt ? 0 : 6); sfor<0, 6>([&](auto jc) AI { u_chk(tt + 1, jc); }); }
        } else {
            IF (tt == 0) sfor<9, 22>([&](auto ic) AI { silu(IC<3>{}, ic); });
            IF (tt == 2) { u_ld1(3, IC<0>{}); u_ld1(3, IC<1>{}); }
            IF (tt > 0) sfor<2, 15>([&](auto ic) AI { fin_out(IC<tt - 1>{}, ic); });
            IF (tt < 2) { VMW(tt ? 1 : 2); sfor<0, 6>([&](auto jc) AI { u_chk(tt + 1, jc); }); }
            SB();
            sfor<0, 20>([&](auto ic) AI {
                CI(i, ic); constexpr int c = (i / 10) * 2 + (i & 1), s = (i % 10) >> 1;
                a2[c] = mfma32(W2[s][c], __builtin_bit_cast(bf16x8, u[s]), a2[c]);
                IF (tt == 0 && i % 3 == 0 && i < 15) u_ld1(2, IC<i / 3>{});
                IF (tt == 2 && i % 3 == 0 && i < 9) u_ld1(3, IC<2 + i / 3>{});
                IF (i == 13) pwr(0);
                IF (i == 16) pwr(1);
                SB();
            });
            pwr(2); pwr(3);
        }
        BAR();
        STAMP(31 + tt * 2);
    });
    if (B) {
        sfor<0, 15>([&](auto ic) AI { fin_out(IC<3>{}, ic); });
    }
    STAMP(38); STAMPC(63);
    IF (B) asm volatile(".Lpfe:");
    IF (A) asm volatile(".Lpfa:");
    __builtin_amdgcn_endpgm();
    };
    if (kh == 0) body(IC<0>{});
    body(IC<1>{});
}

#if defined(__HIP_DEVICE_COMPILE__) && !defined(PK)
#pragma clang attribute pop
#endif
extern "C" void kernel_launch(void* const* d_in, const int* in_sizes, int n_in, void* d_out, int out_size, void* d_ws, size_t ws_size, hipStream_t stream) {
    fused_block<<<256, 512, 0, stream>>>((const char*)d_in[0], (const char*)d_in[1], (const char*)d_in[2], (const char*)d_in[4], (const char*)d_in[5],
                                         (const char*)d_in[6], (const char*)d_in[7], (char*)d_ws, (char*)d_out);
}
```
